```python
import torch

def _softmax(x, axis=-1):
    return torch.softmax(x, dim=axis)
def _gelu(x):
    return torch.nn.functional.gelu(x, approximate="tanh")
def _silu(x):
    return torch.nn.functional.silu(x)
def _sigmoid(x):
    return torch.sigmoid(x)
def _layernorm(x, axis=-1, eps=1e-5):
    m = x.mean(dim=axis, keepdim=True)
    v = x.var(dim=axis, keepdim=True, unbiased=False)
    return (x - m) / torch.sqrt(v + eps)
def _scatter_add(tgt, idx, src, axis=0):
    return tgt.clone().index_add_(axis, idx, src)
def _segment_sum(src, seg, n):
    out = torch.zeros((n,) + src.shape[1:], dtype=src.dtype)
    return out.index_add_(0, seg, src)
def _pad(x, pw):
    flat = tuple(v for pair in reversed(pw) for v in pair)
    return torch.nn.functional.pad(x, flat)

kernel_name = 'gated_mix__linear_kernel_hedgehog__mixer_token_mlp__prompt'

def setup_inputs():

    _w = (lambda a: (lambda t: t.to(torch.bfloat16) if t.is_floating_point() else t)(torch.as_tensor(a)))
    def _A_setup():
        B, S, D, H = 1, 2048, 1024, 32
        Dh = D // H
        return {
            "x": _w((lambda *s: torch.randn(*s, dtype=torch.bfloat16) * 0.05)(B, S, D)),
            "wqkv": _w((lambda *s: torch.randn(*s, dtype=torch.bfloat16) * 0.8)(D, 3 * D)),
            "w_phi": _w((lambda *s: torch.randn(*s, dtype=torch.bfloat16) * 0.8)(H, Dh, Dh) * (Dh ** -0.5)),
            "wo": _w((lambda *s: torch.randn(*s, dtype=torch.bfloat16) * 0.05)(D, D)),
        }
    _A_in = _A_setup()

    def _B_setup():
        B, S, D, F = 1, 2048, 1024, 3584
        return {
            "x": _w((lambda *s: torch.randn(*s, dtype=torch.bfloat16) * 0.05)(B, S, D)),
            "w1": _w((lambda *s: torch.randn(*s, dtype=torch.bfloat16) * 0.8)(S, F) * (S ** -0.5)),
            "b1": _w((lambda *s: torch.randn(*s, dtype=torch.bfloat16) * 12.8)(F) * 0.02),
            "w2": _w((lambda *s: torch.randn(*s, dtype=torch.bfloat16) * 0.05)(F, S) * (F ** -0.5)),
            "b2": _w((lambda *s: torch.randn(*s, dtype=torch.bfloat16) * 0.8)(S) * 0.02),
        }
    _B_in = _B_setup()

    return {
        "A_x": _A_in["x"],
        "A_wqkv": _A_in["wqkv"],
        "A_w_phi": _A_in["w_phi"],
        "A_wo": _A_in["wo"],
        "B_x": _B_in["x"],
        "B_w1": _B_in["w1"],
        "B_b1": _B_in["b1"],
        "B_w2": _B_in["w2"],
        "B_b2": _B_in["b2"],
        "gate_w": _w((lambda *s: torch.randn(*s, dtype=torch.bfloat16) * 0.05)(1024) * 16.0),
    }

def reference(A_x, A_wqkv, A_w_phi, A_wo, B_x, B_w1, B_b1, B_w2, B_b2, gate_w):
    def _A_ref(x, wqkv, w_phi, wo):
        B, S, D = x.shape
        H = 32; Dh = D // H
        qkv = (lambda x, *d: x.permute(*d))((x @ wqkv).reshape(B, S, 3, H, Dh), 2, 0, 3, 1, 4)
        q, k, v = qkv[0], qkv[1], qkv[2]
        pq = torch.einsum("bhsd,hdf->bhsf", q, w_phi)
        pk = torch.einsum("bhsd,hdf->bhsf", k, w_phi)
        phi_q = torch.concatenate([_softmax(pq, axis=-1), _softmax(-pq, axis=-1)], axis=-1)
        phi_k = torch.concatenate([_softmax(pk, axis=-1), _softmax(-pk, axis=-1)], axis=-1)
        kv = torch.einsum("bhsf,bhsd->bhfd", phi_k, v)
        z  = torch.einsum("bhsf->bhf", phi_k)
        num = torch.einsum("bhqf,bhfd->bhqd", phi_q, kv)
        den = torch.einsum("bhqf,bhf->bhq", phi_q, z)[..., None] + 1e-6
        o = num / den
        return (lambda x, *d: x.permute(*d))(o, 0, 2, 1, 3).reshape(B, S, D) @ wo

    def _B_ref(x, w1, b1, w2, b2):
        B, S, D = x.shape
        h = _layernorm(x, axis=-1)
        u = torch.einsum("bsd,sf->bdf", h, w1) + b1
        u = _gelu(u)
        y = torch.einsum("bdf,fs->bds", u, w2) + b2
        return (lambda x, *d: x.permute(*d))(y, 0, 2, 1)

    x = A_x
    g = _sigmoid((x * gate_w).sum(-1, keepdims=True))
    a = _A_ref(x, A_wqkv, A_w_phi, A_wo)
    b = _B_ref(x, B_w1, B_b1, B_w2, B_b2)
    return g * a + (1 - g) * b
```

```cpp
#include <hip/hip_runtime.h>
#include <hip/hip_bf16.h>
typedef unsigned short u16;
__device__ __forceinline__ float bf2f(u16 b){ return __uint_as_float(((unsigned)b)<<16); }
__device__ __forceinline__ u16 f2bf(float f){ unsigned u=__float_as_uint(f); u += 0x7FFFu + ((u>>16)&1u); return (u16)(u>>16); }
constexpr int S=2048, D=1024, H=32, DH=32, F=3584;

template<int MODE>
__global__ void gemm_naive(const u16* A, long sam, long sak, const u16* B, long sbk, long sbn, float* C, int M, int N, int K){
  __shared__ float As[16][17], Bs[16][17];
  int tx=threadIdx.x&15, ty=threadIdx.x>>4;
  int m=blockIdx.y*16+ty, n=blockIdx.x*16+tx;
  float acc=0;
  for(int k0=0;k0<K;k0+=16){
    As[ty][tx]=bf2f(A[(long)(blockIdx.y*16+ty)*sam+(long)(k0+tx)*sak]);
    Bs[ty][tx]=bf2f(B[(long)(k0+ty)*sbk+(long)(blockIdx.x*16+tx)*sbn]);
    __syncthreads();
    for(int k=0;k<16;++k) acc+=As[ty][k]*Bs[k][tx];
    __syncthreads();
  }
  C[(long)m*N+n]=acc;
}
__global__ void ln_gate(const u16* x, const u16* gw, u16* h, float* g){
  int s=blockIdx.x; __shared__ float red[3][256];
  int t=threadIdx.x; float v[4]; float sum=0, gs=0;
  for(int i=0;i<4;++i){ v[i]=bf2f(x[s*D+t*4+i]); sum+=v[i]; gs+=bf2f(f2bf(v[i]*bf2f(gw[t*4+i]))); }
  red[0][t]=sum; red[1][t]=gs; __syncthreads();
  for(int o=128;o>0;o>>=1){ if(t<o){red[0][t]+=red[0][t+o]; red[1][t]+=red[1][t+o];} __syncthreads(); }
  float mean=red[0][0]/D; float gsum=red[1][0]; __syncthreads();
  float q=0; for(int i=0;i<4;++i){ float d=v[i]-mean; q+=d*d; }
  red[2][t]=q; __syncthreads();
  for(int o=128;o>0;o>>=1){ if(t<o){red[2][t]+=red[2][t+o];} __syncthreads(); }
  float rstd=1.0f/sqrtf(red[2][0]/D+1e-5f);
  for(int i=0;i<4;++i) h[s*D+t*4+i]=f2bf((v[i]-mean)*rstd);
  if(t==0) g[s]=1.0f/(1.0f+__expf(-gsum));
}
__global__ void phi_k(const float* qkv, const u16* wphi, u16* phiq, u16* phik, u16* vb){
  int s=blockIdx.x, h=threadIdx.x>>5, f=threadIdx.x&31;
  __shared__ float q[2][H][DH]; __shared__ float p[2][H][DH];
  q[0][h][f]=bf2f(f2bf(qkv[(long)s*3072+h*32+f])); q[1][h][f]=bf2f(f2bf(qkv[(long)s*3072+1024+h*32+f]));
  vb[(long)s*D+h*32+f]=f2bf(qkv[(long)s*3072+2048+h*32+f]);
  __syncthreads();
  for(int w=0;w<2;++w){ float a=0; for(int d=0;d<32;++d) a+=q[w][h][d]*bf2f(wphi[(h*32+d)*32+f]); p[w][h][f]=bf2f(f2bf(a)); }
  __syncthreads();
  for(int w=0;w<2;++w){ float mx=-1e30f,mn=1e30f; for(int d=0;d<32;++d){mx=fmaxf(mx,p[w][h][d]); mn=fminf(mn,p[w][h][d]);}
    float s1=0,s2=0; for(int d=0;d<32;++d){ s1+=__expf(p[w][h][d]-mx); s2+=__expf(mn-p[w][h][d]); }
    u16* o=(w? phik:phiq)+((long)s*H+h)*64;
    o[f]=f2bf(__expf(p[w][h][f]-mx)/s1); o[32+f]=f2bf(__expf(mn-p[w][h][f])/s2); }
}
__global__ void kv_k(const u16* phik, const u16* vb, float* kv, float* z){
  int h=blockIdx.x, f=blockIdx.y; int d=threadIdx.x&31, part=threadIdx.x>>5;
  __shared__ float red[8][33];
  float a=0, zz=0; for(int s=part;s<S;s+=8){ float ph=bf2f(phik[((long)s*H+h)*64+f]); a+=ph*bf2f(vb[(long)s*D+h*32+d]); zz+=ph; }
  red[part][d]=a; if(d==0) red[part][32]=zz; __syncthreads();
  if(part==0){ float t=0; for(int i=0;i<8;++i) t+=red[i][d]; kv[(h*64+f)*32+d]=bf2f(f2bf(t)); if(d==0){ float tz=0; for(int i=0;i<8;++i) tz+=red[i][32]; z[h*64+f]=bf2f(f2bf(tz)); } }
}
__global__ void o_k(const u16* phiq, const float* kv, const float* z, u16* o){
  int s=blockIdx.x, h=threadIdx.x>>5, d=threadIdx.x&31;
  float num=0, den=0; for(int f=0;f<64;++f){ float ph=bf2f(phiq[((long)s*H+h)*64+f]); num+=ph*kv[(h*64+f)*32+d]; den+=ph*z[h*64+f]; }
  o[(long)s*D+h*32+d]=f2bf(bf2f(f2bf(num))/(bf2f(f2bf(den))+1e-6f));
}
__global__ void gelu_k(const float* uf, const u16* b1, u16* u){
  long i=(long)blockIdx.x*256+threadIdx.x; int f=i%F; float x=bf2f(f2bf(uf[i]))+bf2f(b1[f]); x=bf2f(f2bf(x));
  float y=0.7978845608f*(x+0.044715f*x*x*x); u[i]=f2bf(0.5f*x*(1.0f+tanhf(y)));
}
__global__ void fin_k(const float* a, const float* y, const u16* b2, const float* g, u16* out){
  long i=(long)blockIdx.x*256+threadIdx.x; int s=i/D, d=i%D; float b=y[(long)d*S+s]+bf2f(b2[s]);
  out[i]=f2bf(g[s]*a[i]+(1.0f-g[s])*b);
}
extern "C" void kernel_launch(void* const* d_in, const int* in_sizes, int n_in, void* d_out, int out_size, void* d_ws, size_t ws_size, hipStream_t stream){
  const u16* x=(const u16*)d_in[0]; const u16* wqkv=(const u16*)d_in[1]; const u16* wphi=(const u16*)d_in[2]; const u16* wo=(const u16*)d_in[3];
  const u16* w1=(const u16*)d_in[5]; const u16* b1=(const u16*)d_in[6]; const u16* w2=(const u16*)d_in[7]; const u16* b2=(const u16*)d_in[8]; const u16* gw=(const u16*)d_in[9];
  char* ws=(char*)d_ws; size_t o=0; auto al=[&](size_t n){ char* p=ws+o; o+=(n+255)&~(size_t)255; return p; };
  u16* h=(u16*)al(S*D*2); float* g=(float*)al(S*4); float* qkv=(float*)al((size_t)S*3072*4); u16* phiq=(u16*)al((size_t)S*H*64*2); u16* phik=(u16*)al((size_t)S*H*64*2);
  u16* vb=(u16*)al(S*D*2); float* kv=(float*)al(H*64*32*4); float* z=(float*)al(H*64*4); u16* ob=(u16*)al(S*D*2); float* a=(float*)al(S*D*4);
  float* uf=(float*)al((size_t)D*F*4); u16* u=(u16*)al((size_t)D*F*2); float* y=(float*)al((size_t)D*S*4);
  ln_gate<<<S,256,0,stream>>>(x,gw,h,g);
  gemm_naive<0><<<dim3(3072/16,S/16),256,0,stream>>>(x,D,1,wqkv,3072,1,qkv,S,3072,D);
  phi_k<<<S,1024,0,stream>>>(qkv,wphi,phiq,phik,vb);
  kv_k<<<dim3(H,64),256,0,stream>>>(phik,vb,kv,z);
  o_k<<<S,1024,0,stream>>>(phiq,kv,z,ob);
  gemm_naive<0><<<dim3(D/16,S/16),256,0,stream>>>(ob,D,1,wo,D,1,a,S,D,D);
  gemm_naive<0><<<dim3(F/16,D/16),256,0,stream>>>(h,1,D,w1,F,1,uf,D,F,S);
  gelu_k<<<D*F/256,256,0,stream>>>(uf,b1,u);
  gemm_naive<0><<<dim3(S/16,D/16),256,0,stream>>>(u,F,1,w2,S,1,y,D,S,F);
  fin_k<<<S*D/256,256,0,stream>>>(a,y,b2,g,(u16*)d_out);
}
```

```cpp
#include <hip/hip_runtime.h>
#include <hip/hip_bf16.h>

typedef unsigned short u16;
typedef short bf16x8 __attribute__((ext_vector_type(8)));
typedef short s16x4 __attribute__((ext_vector_type(4)));
typedef float f32x2 __attribute__((ext_vector_type(2)));
typedef float f32x4 __attribute__((ext_vector_type(4)));
typedef float f32x16 __attribute__((ext_vector_type(16)));
typedef unsigned u32x4 __attribute__((ext_vector_type(4)));
typedef unsigned u32x2 __attribute__((ext_vector_type(2)));
typedef __bf16 bf16x2_t __attribute__((ext_vector_type(2)));
#define LAS __attribute__((address_space(3)))
#define GAS __attribute__((address_space(1)))
#define DI __device__ __forceinline__

constexpr int S = 2048, D = 1024, H = 32, F = 3584;

constexpr size_t WS_FLAGS = 0;
constexpr size_t WS_GATE  = 65536;
constexpr size_t WS_HT    = 1u << 20;
constexpr size_t WS_UT    = WS_HT + (4u << 20);
constexpr size_t WS_OT    = WS_UT + (8u << 20);
constexpr size_t WS_KVP   = WS_OT + (4u << 20);
constexpr size_t WS_ZP    = WS_KVP + (2u << 20);
constexpr size_t WS_PHQ   = WS_ZP + (1u << 20);
constexpr size_t WS_XCH   = WS_PHQ + (8u << 20);

struct Params {
  const u16 *x, *wqkv, *wphi, *wo, *w1, *b1, *w2, *b2, *gw;
  char* ws; u16* out;
};

DI float bf2f(u16 b) { return __uint_as_float(((unsigned)b) << 16); }
DI float bflo(unsigned w) { return __uint_as_float(w << 16); }
DI float bfhi(unsigned w) { return __uint_as_float(w & 0xffff0000u); }
DI unsigned pk2(float a, float b) { bf16x2_t v = {(__bf16)a, (__bf16)b}; return __builtin_bit_cast(unsigned, v); }
DI float rbf(float a) { return __uint_as_float(pk2(a, a) << 16); }
DI bf16x8 mk8(unsigned a, unsigned b, unsigned c, unsigned d) { u32x4 v = {a, b, c, d}; return __builtin_bit_cast(bf16x8, v); }
DI f32x4 sel4(bool c, f32x4 a, f32x4 b) { f32x4 r; r[0] = c ? a[0] : b[0]; r[1] = c ? a[1] : b[1]; r[2] = c ? a[2] : b[2]; r[3] = c ? a[3] : b[3]; return r; }
DI bf16x8 cat8(s16x4 lo, s16x4 hi) { return __builtin_shufflevector(lo, hi, 0, 1, 2, 3, 4, 5, 6, 7); }

template <int IMM, bool SC1> DI void dma16(unsigned voff, const void* sbase, unsigned ldsaddr) {
  if (SC1) asm volatile("s_mov_b32 m0, %2\n\ts_nop 0\n\tglobal_load_lds_dwordx4 %0, %1 offset:%3 sc1" ::"v"(voff), "s"(sbase), "s"(ldsaddr), "i"(IMM) : "memory");
  else asm volatile("s_mov_b32 m0, %2\n\ts_nop 0\n\tglobal_load_lds_dwordx4 %0, %1 offset:%3" ::"v"(voff), "s"(sbase), "s"(ldsaddr), "i"(IMM) : "memory");
}
#define WAIT_V(n) asm volatile("s_waitcnt vmcnt(" #n ")" ::: "memory")
#define WAIT_L0() asm volatile("s_waitcnt lgkmcnt(0)" ::: "memory")
#define BAR() do { asm volatile("s_barrier" ::: "memory"); __builtin_amdgcn_sched_barrier(0); } while (0)
#define LBAR() do { asm volatile("s_waitcnt lgkmcnt(0)\n\ts_barrier" ::: "memory"); __builtin_amdgcn_sched_barrier(0); } while (0)
DI s16x4 trd(LAS char* p) { return __builtin_amdgcn_ds_read_tr16_b64_v4i16((LAS s16x4*)p); }
DI bf16x8 rd8(LAS char* p) { return *(LAS bf16x8*)p; }
#define MFMA16(a, b, c) __builtin_amdgcn_mfma_f32_16x16x32_bf16((a), (b), (c), 0, 0, 0)
#define MFMA32(a, b, c) __builtin_amdgcn_mfma_f32_32x32x16_bf16((a), (b), (c), 0, 0, 0)

DI float dppf(float v, int) { return v; }
template <int CTRL> DI float dpp_mov(float v) { return __builtin_bit_cast(float, __builtin_amdgcn_update_dpp(0, __builtin_bit_cast(int, v), CTRL, 0xF, 0xF, true)); }
DI float xor32(float v) {
  unsigned u = __float_as_uint(v); auto r = __builtin_amdgcn_permlane32_swap(u, u, false, false);
  return __uint_as_float((threadIdx.x & 32) ? r[0] : r[1]);
}
DI float xor16(float v) {
  unsigned u = __float_as_uint(v); auto r = __builtin_amdgcn_permlane16_swap(u, u, false, false);
  return __uint_as_float((threadIdx.x & 16) ? r[0] : r[1]);
}
DI float wave_sum(float v) {
  v += dpp_mov<0xB1>(v);
  v += dpp_mov<0x4E>(v);
  v += dpp_mov<0x141>(v);
  v += dpp_mov<0x140>(v);
  v += xor16(v);
  v += xor32(v);
  return v;
}

DI void ln_unit(const Params& P, LAS char* lds, int m) {
  const int tid = threadIdx.x, lane = tid & 63, w = tid >> 6;
  const int kb = m >> 1, hf = m & 1;
  LAS char* xs = lds;
  LAS f32x2* st = (LAS f32x2*)(lds + 32768);
  const u32x4 g0 = *(const u32x4*)(P.gw + lane * 8), g1 = *(const u32x4*)(P.gw + 512 + lane * 8);
  u32x4 v[2][2];
#pragma unroll
  for (int e = 0; e < 2; ++e) {
    const int i = 2 * w + e, tok = 32 * kb + (i < 8 ? 8 * hf + i : 16 + 8 * hf + (i - 8));
    v[e][0] = *(const u32x4*)(P.x + (size_t)tok * D + lane * 8);
    v[e][1] = *(const u32x4*)(P.x + (size_t)tok * D + 512 + lane * 8);
  }
#pragma unroll
  for (int e = 0; e < 2; ++e) {
    const int i = 2 * w + e, tok = 32 * kb + (i < 8 ? 8 * hf + i : 16 + 8 * hf + (i - 8));
    float s = 0.f, q = 0.f, gd = 0.f;
#pragma unroll
    for (int c = 0; c < 2; ++c)
#pragma unroll
      for (int j = 0; j < 4; ++j) {
        const unsigned xw = v[e][c][j], gw = c ? g1[j] : g0[j];
        const float a = bflo(xw), b = bfhi(xw);
        s += a + b; q += a * a + b * b; gd += a * bflo(gw) + b * bfhi(gw);
      }
    s = wave_sum(s); q = wave_sum(q); gd = wave_sum(gd);
    const float mean = s * (1.0f / D);
    const float var = fmaxf(q * (1.0f / D) - mean * mean, 0.f);
    const float rstd = 1.0f / sqrtf(var + 1e-5f);
    *(LAS u32x4*)(xs + i * 2048 + lane * 16) = v[e][0];
    *(LAS u32x4*)(xs + i * 2048 + 1024 + lane * 16) = v[e][1];
    if (lane == 0) { st[i] = (f32x2){mean, rstd}; ((float*)(P.ws + WS_GATE))[tok] = 1.0f / (1.0f + __expf(-gd)); }
  }
  __syncthreads();
  float h0[16], h1[16];
#pragma unroll
  for (int i = 0; i < 16; ++i) {
    const unsigned xw = *(LAS unsigned*)(xs + i * 2048 + tid * 4); const f32x2 s = st[i];
    h0[i] = (bflo(xw) - s.x) * s.y; h1[i] = (bfhi(xw) - s.x) * s.y;
  }
  const int d = 2 * tid;
  char* dst = P.ws + WS_HT + ((size_t)(kb * 64 + (d >> 4)) * 64 + (d & 15)) * 16;
#pragma unroll
  for (int e = 0; e < 2; ++e) {
    const int kg = 2 * hf + e, a = 4 * e, b = 8 + 4 * e;
    u32x4 c0 = {pk2(h0[a], h0[a + 1]), pk2(h0[a + 2], h0[a + 3]), pk2(h0[b], h0[b + 1]), pk2(h0[b + 2], h0[b + 3])};
    u32x4 c1 = {pk2(h1[a], h1[a + 1]), pk2(h1[a + 2], h1[a + 3]), pk2(h1[b], h1[b + 1]), pk2(h1[b + 2], h1[b + 3])};
    *(u32x4*)(dst + kg * 256) = c0; *(u32x4*)(dst + kg * 256 + 16) = c1;
  }
}

namespace m1 { constexpr int SLOT = 24576, NS = 6, NSL = 64; }
DI float gelu_tanh(float x) {
  const float y = 0.7978845608028654f * (x + 0.044715f * x * x * x);
  const float e = __builtin_amdgcn_exp2f(-2.885390081777927f * y);
  return x * __builtin_amdgcn_rcpf(1.0f + e);
}
DI void mlp1_tile(const Params& P, LAS char* lds, int dt, int ft) {
  using namespace m1;
  const int tid = threadIdx.x, lane = tid & 63;
  const int wid = __builtin_amdgcn_readfirstlane(tid >> 6);
  const int g = wid >> 2, w4 = wid & 3, wm = w4 >> 1, wn = w4 & 1, odd = ft & 1;
  const char* wsrc = (const char*)P.w1 + (224 * ft - 32 * odd) * 2;
  const char* hsrc = P.ws + WS_HT + (size_t)(dt * 8 + 2 * w4) * 1024;
  const int r0 = 2 * w4 + (lane >> 5), c = lane & 31;
  const unsigned voffW = r0 * 7168 + ((((c >> 1) ^ r0) & 15) << 5) + ((c & 1) << 4) + 0;
  const unsigned voffH = lane * 16;
  auto issue = [&](int j) {
    const unsigned sb = (unsigned)(j % NS) * SLOT;
    const char* ws_ = wsrc + (size_t)j * (32 * 7168);
#pragma unroll
    for (int i = 0; i < 4; ++i) dma16<0, false>(voffW, ws_ + i * (8 * 7168), sb + (w4 + 4 * i) * 1024);
    const char* hs_ = hsrc + (size_t)j * 65536;
    dma16<0, true>(voffH, hs_, sb + 16384 + 2 * w4 * 1024);
    dma16<1024, true>(voffH, hs_, sb + 16384 + 2 * w4 * 1024);
  };
  const int kg = lane >> 4, q = (lane & 15) >> 2, p = lane & 3, r7 = 4 * (kg & 1) + q;
  const unsigned base0 = (4 * kg + q) * 512 + (r7 << 5) + 8 * p;
  const int u0 = 7 * wn + 2 * odd;
  unsigned offW[7];
#pragma unroll
  for (int i = 0; i < 7; ++i) offW[i] = base0 ^ ((u0 + i) << 5);
  const unsigned offH = 16384 + 4 * wm * 1024 + lane * 16;

  f32x4 acc[7][4];
#pragma unroll
  for (int i = 0; i < 7; ++i)
#pragma unroll
    for (int d = 0; d < 4; ++d) acc[i][d] = (f32x4){0.f, 0.f, 0.f, 0.f};

  issue(g); issue(g + 2);
  WAIT_V(6); BAR();
  if (g == 1) BAR();
  for (int j = g; j < NSL; j += 2) {
    LAS char* sl = lds + (j % NS) * SLOT;
    bf16x8 wf[7], hf[4];
#pragma unroll
    for (int i = 0; i < 7; ++i) wf[i] = cat8(trd(sl + offW[i]), trd(sl + offW[i] + 8192));
#pragma unroll
    for (int d = 0; d < 4; ++d) hf[d] = rd8(sl + offH + d * 1024);
    if (j + 4 < NSL) issue(j + 4);
    LBAR();
    __builtin_amdgcn_s_setprio(1);
#pragma unroll
    for (int i = 0; i < 7; ++i)
#pragma unroll
      for (int d = 0; d < 4; ++d) acc[i][d] = MFMA16(wf[i], hf[d], acc[i][d]);
    __builtin_amdgcn_s_setprio(0);
    if (j + 4 < NSL) WAIT_V(6); else WAIT_V(0);
    BAR();
  }
  if (g == 0) BAR();
  LAS char* X = lds;
  auto xaddr = [&](int w, int e) { return X + (w * 15 + e) * 1024 + lane * 16; };
  const int og = 1 - g;
#pragma unroll
  for (int dd = 0; dd < 2; ++dd)
#pragma unroll
    for (int i = 0; i < 7; ++i) {
      const f32x4 v = sel4(g, acc[i][dd], acc[i][2 + dd]);
      *(LAS f32x4*)xaddr(wid, dd * 7 + i) = v;
    }
  {
    const f32x4 a = sel4(wn, sel4(g, acc[0][2], acc[0][0]), sel4(g, acc[6][3], acc[6][1]));
    *(LAS f32x4*)xaddr(wid, 14) = a;
  }
  LBAR();
  const int wP = wid ^ 4, wQ0 = wid ^ 1, wQ1 = wid ^ 5;
  const char* utile = P.ws + WS_UT + (size_t)(7 * ft) * 65536 + (size_t)(dt * 8 + 4 * wm + 2 * g) * 1024 + lane * 16;
  const u16* b1p = P.b1 + 224 * ft + 4 * kg;
  auto fin = [&](f32x4 v, int b) -> u32x2 {
    const u32x2 bw = *(const u32x2*)(b1p + 16 * b);
    const float r0_ = gelu_tanh(v[0] + bflo(bw[0])), r1_ = gelu_tanh(v[1] + bfhi(bw[0])), r2_ = gelu_tanh(v[2] + bflo(bw[1])), r3_ = gelu_tanh(v[3] + bfhi(bw[1]));
    return (u32x2){pk2(r0_, r1_), pk2(r2_, r3_)};
  };
#pragma unroll
  for (int dd = 0; dd < 2; ++dd)
#pragma unroll
    for (int pr = 0; pr < 3; ++pr) {
      const f32x4 o0 = sel4(wn, sel4(g, acc[2 * pr + 1][2 + dd], acc[2 * pr + 1][dd]), sel4(g, acc[2 * pr][2 + dd], acc[2 * pr][dd]));
      const f32x4 o1 = sel4(wn, sel4(g, acc[2 * pr + 2][2 + dd], acc[2 * pr + 2][dd]), sel4(g, acc[2 * pr + 1][2 + dd], acc[2 * pr + 1][dd]));
      const int i0 = 2 * pr + wn;
      const f32x4 p0 = *(LAS f32x4*)xaddr(wP, dd * 7 + i0), p1 = *(LAS f32x4*)xaddr(wP, dd * 7 + i0 + 1);
      const int b = 7 * wn + i0;
      const u32x2 lo = fin(o0 + p0, b), hi = fin(o1 + p1, b + 1);
      *(u32x4*)(utile + (size_t)(b >> 1) * 65536 + dd * 1024) = (u32x4){lo[0], lo[1], hi[0], hi[1]};
    }
  {
    const f32x4 own = sel4(wn, sel4(g, acc[0][3], acc[0][1]), sel4(g, acc[6][2], acc[6][0]));
    const int im = wn ? 0 : 6, imq = wn ? 6 : 0;
    const f32x4 mine = own + *(LAS f32x4*)xaddr(wP, wn * 7 + im);
    const f32x4 oth = *(LAS f32x4*)xaddr(wQ0, 14) + *(LAS f32x4*)xaddr(wQ1, wn * 7 + imq);
    const f32x4 b6 = sel4(wn, oth, mine), b7 = sel4(wn, mine, oth);
    const u32x2 lo = fin(b6, 6), hi = fin(b7, 7);
    *(u32x4*)(utile + (size_t)3 * 65536 + wn * 1024) = (u32x4){lo[0], lo[1], hi[0], hi[1]};
  }
  (void)og;
}

namespace qk { constexpr int SLOT = 57344, WOFF = 32768, NT = 16; }
DI void softmax_pm(const f32x16& pq, u32x4 (&out)[4]) {
  float p[16];
#pragma unroll
  for (int i = 0; i < 16; i += 2) { const unsigned w = pk2(pq[i], pq[i + 1]); p[i] = bflo(w); p[i + 1] = bfhi(w); }
  float mx = p[0], mn = p[0];
#pragma unroll
  for (int i = 1; i < 16; ++i) { mx = fmaxf(mx, p[i]); mn = fminf(mn, p[i]); }
  mx = fmaxf(mx, xor32(mx)); mn = fminf(mn, xor32(mn));
  constexpr float L2E = 1.4426950408889634f;
  const float mxs = mx * L2E, mns = mn * L2E;
  float ep[16], en[16], sp = 0.f, sn = 0.f;
#pragma unroll
  for (int i = 0; i < 16; ++i) {
    ep[i] = __builtin_amdgcn_exp2f(p[i] * L2E - mxs); en[i] = __builtin_amdgcn_exp2f(mns - p[i] * L2E);
    sp += ep[i]; sn += en[i];
  }
  sp += xor32(sp); sn += xor32(sn);
  const float rp = __builtin_amdgcn_rcpf(sp), rn = __builtin_amdgcn_rcpf(sn);
#pragma unroll
  for (int s = 0; s < 2; ++s)
#pragma unroll
    for (int j = 0; j < 4; ++j) {
      out[s][j] = pk2(ep[8 * s + 2 * j] * rp, ep[8 * s + 2 * j + 1] * rp);
      out[2 + s][j] = pk2(en[8 * s + 2 * j] * rn, en[8 * s + 2 * j + 1] * rn);
    }
}
DI bf16x8 pack8(const f32x16& x, int s) {
  return mk8(pk2(x[8 * s], x[8 * s + 1]), pk2(x[8 * s + 2], x[8 * s + 3]), pk2(x[8 * s + 4], x[8 * s + 5]), pk2(x[8 * s + 6], x[8 * s + 7]));
}
DI void qkv_tile(const Params& P, LAS char* lds, int st, int hp) {
  using namespace qk;
  const int tid = threadIdx.x, lane = tid & 63;
  const int wid = __builtin_amdgcn_readfirstlane(tid >> 6);
  const int g = wid >> 2, w4 = wid & 3, wm2 = w4 >> 1, wn = w4 & 1;
  const int head = 2 * hp + wn, tokb = 128 * g + 64 * wm2;
  const int h2 = lane >> 5, r = lane & 31;
  bf16x8 wphi[2];
#pragma unroll
  for (int s = 0; s < 2; ++s)
#pragma unroll
    for (int j = 0; j < 8; ++j) wphi[s][j] = (short)P.wphi[(head * 32 + 16 * s + 8 * (j >> 2) + 4 * h2 + (j & 3)) * 32 + r];
  unsigned voffW[3];
#pragma unroll
  for (int b = 0; b < 3; ++b) {
    const int li = 8 * b + (lane >> 3), row = li / 3, part = li % 3;
    voffW[b] = row * 6144 + part * 2048 + (((lane & 7) ^ (((row >> 1) & 1) << 2)) << 4);
  }
  const unsigned voffX = (lane >> 3) * 2048 + (((lane & 7) ^ ((4 * (w4 & 1) + (lane >> 4)) & 7)) << 4);
  const char* wsrc = (const char*)P.wqkv + hp * 128;
  const char* xsrc = (const char*)P.x + (size_t)(256 * st) * 2048;
  auto issueW = [&](int t) {
    const unsigned sb = (t & 1) * SLOT + WOFF + 6 * w4 * 1024;
    const char* s_ = wsrc + (size_t)(64 * t + 16 * w4) * 6144;
#pragma unroll
    for (int i = 0; i < 6; ++i) dma16<0, false>(voffW[i % 3], s_ + (i / 3) * (8 * 6144), sb + i * 1024);
  };
  auto issueX = [&](int t, int hi) {
    const unsigned sb = (t & 1) * SLOT + (16 * hi + w4) * 1024;
    const char* s_ = xsrc + (size_t)(16 * hi + w4) * 16384 + t * 128;
#pragma unroll
    for (int i = 0; i < 4; ++i) dma16<0, false>(voffX, s_ + i * (4 * 16384), sb + i * 4096);
  };
  unsigned offX[4];
#pragma unroll
  for (int s = 0; s < 4; ++s) offX[s] = (tokb + r) * 128 + (((2 * s + h2) ^ ((r >> 1) & 7)) << 4);
  const int blk = (lane >> 4) & 1, q = (lane & 15) >> 2, p = lane & 3;
  const unsigned offW = WOFF + (8 * h2 + q) * 384 + 64 * (wn ^ (q >> 1)) + 32 * blk + 8 * p;

  f32x16 aq[2], ak[2], av[2];
#pragma unroll
  for (int t = 0; t < 2; ++t)
#pragma unroll
    for (int i = 0; i < 16; ++i) { aq[t][i] = 0.f; ak[t][i] = 0.f; av[t][i] = 0.f; }

  if (g == 0) { issueW(0); WAIT_V(0); }
  else { issueX(0, 0); issueX(0, 1); issueX(1, 0); WAIT_V(8); }
  BAR();
  if (g == 1) { WAIT_V(4); BAR(); }
  for (int t = 0; t < NT; ++t) {
    LAS char* sl = lds + (t & 1) * SLOT;
    bf16x8 xf[2][4], wq[4], wk[4], wv[4];
#pragma unroll
    for (int s = 0; s < 4; ++s) {
      wq[s] = cat8(trd(sl + offW + (16 * s) * 384), trd(sl + offW + (16 * s + 4) * 384));
      wk[s] = cat8(trd(sl + offW + (16 * s) * 384 + 128), trd(sl + offW + (16 * s + 4) * 384 + 128));
      wv[s] = cat8(trd(sl + offW + (16 * s) * 384 + 256), trd(sl + offW + (16 * s + 4) * 384 + 256));
      xf[0][s] = rd8(sl + offX[s]); xf[1][s] = rd8(sl + offX[s] + 4096);
    }
    if (g == 0) { if (t + 1 < NT) issueW(t + 1); }
    else {
      if (t + 1 < NT) issueX(t + 1, 1);
      if (t + 2 < NT) issueX(t + 2, 0);
      if (t + 2 < NT) WAIT_V(8); else if (t + 1 < NT) WAIT_V(4);
    }
    LBAR();
    __builtin_amdgcn_s_setprio(1);
#pragma unroll
    for (int s = 0; s < 4; ++s)
#pragma unroll
      for (int tb = 0; tb < 2; ++tb) {
        aq[tb] = MFMA32(wq[s], xf[tb][s], aq[tb]);
        ak[tb] = MFMA32(wk[s], xf[tb][s], ak[tb]);
        av[tb] = MFMA32(xf[tb][s], wv[s], av[tb]);
      }
    __builtin_amdgcn_s_setprio(0);
    if (g == 0) WAIT_V(0);
    else { if (t + 2 < NT) WAIT_V(4); else WAIT_V(0); }
    BAR();
  }
  if (g == 0) BAR();
  LAS char* img = lds + wid * 8192;
  char* phq = P.ws + WS_PHQ + ((size_t)((st * 16 + hp) * 8 + wid) * 8) * 1024 + lane * 16;
  f32x16 zero;
#pragma unroll
  for (int i = 0; i < 16; ++i) zero[i] = 0.f;
  const unsigned sw = (r & 1) | (((r >> 2) & 3) << 1);
#pragma unroll
  for (int tb = 0; tb < 2; ++tb) {
    f32x16 pq = MFMA32(wphi[0], pack8(aq[tb], 0), zero); pq = MFMA32(wphi[1], pack8(aq[tb], 1), pq);
    u32x4 ph[4]; softmax_pm(pq, ph);
#pragma unroll
    for (int ks = 0; ks < 4; ++ks) *(u32x4*)(phq + (tb * 4 + ks) * 1024) = ph[ks];
    f32x16 pk = MFMA32(wphi[0], pack8(ak[tb], 0), zero); pk = MFMA32(wphi[1], pack8(ak[tb], 1), pk);
    softmax_pm(pk, ph);
#pragma unroll
    for (int fb = 0; fb < 2; ++fb)
#pragma unroll
      for (int g4 = 0; g4 < 4; ++g4) {
        const u32x2 w = {ph[2 * fb + (g4 >> 1)][2 * (g4 & 1)], ph[2 * fb + (g4 >> 1)][2 * (g4 & 1) + 1]};
        *(LAS u32x2*)(img + (32 * tb + r) * 128 + 64 * (fb ^ ((r >> 1) & 1)) + 8 * ((2 * g4 + h2) ^ sw)) = w;
      }
  }
  WAIT_L0();
  f32x16 kv[2] = {zero, zero}, zz[2] = {zero, zero};
  const bf16x8 ones = mk8(0x3f803f80u, 0x3f803f80u, 0x3f803f80u, 0x3f803f80u);
#pragma unroll
  for (int tb = 0; tb < 2; ++tb)
#pragma unroll
    for (int s = 0; s < 2; ++s) {
      const bf16x8 vb = pack8(av[tb], s);
#pragma unroll
      for (int fb = 0; fb < 2; ++fb) {
        s16x4 a[2];
#pragma unroll
        for (int t = 0; t < 2; ++t) {
          const int row = 16 * s + 8 * t + 4 * h2 + q;
          const unsigned swr = (row & 1) | (((row >> 2) & 3) << 1);
          a[t] = trd(img + (32 * tb + row) * 128 + 64 * (fb ^ ((row >> 1) & 1)) + 8 * ((4 * blk + p) ^ swr));
        }
        const bf16x8 af = cat8(a[0], a[1]);
        kv[fb] = MFMA32(af, vb, kv[fb]); zz[fb] = MFMA32(af, ones, zz[fb]);
      }
    }
  LAS char* red = lds + 65536;
  constexpr int RS = 8448;
#pragma unroll
  for (int fb = 0; fb < 2; ++fb) {
#pragma unroll
    for (int c = 0; c < 4; ++c) *(LAS f32x4*)(red + wid * RS + (fb * 4 + c) * 1024 + lane * 16) = (f32x4){kv[fb][4 * c], kv[fb][4 * c + 1], kv[fb][4 * c + 2], kv[fb][4 * c + 3]};
    if (r == 0) {
#pragma unroll
      for (int c = 0; c < 4; ++c) *(LAS f32x4*)(red + wid * RS + 8192 + (fb * 2 + h2) * 64 + c * 16) = (f32x4){zz[fb][4 * c], zz[fb][4 * c + 1], zz[fb][4 * c + 2], zz[fb][4 * c + 3]};
    }
  }
  LBAR();
  {
    const int qd = 2 * g + wm2;
    float* kvp = (float*)(P.ws + WS_KVP) + (size_t)(st * 32 + head) * 2048;
#pragma unroll
    for (int e = 0; e < 2; ++e) {
      const int ch = 2 * qd + e;
      f32x4 s = (f32x4){0.f, 0.f, 0.f, 0.f};
#pragma unroll
      for (int w = 0; w < 4; ++w) s += *(LAS f32x4*)(red + (2 * w + wn) * RS + ch * 1024 + lane * 16);
      *(f32x4*)(kvp + ((ch >> 2) * 64 + lane) * 16 + 4 * (ch & 3)) = s;
    }
    if (qd == 0 && lane < 16) {
      f32x4 s = (f32x4){0.f, 0.f, 0.f, 0.f};
#pragma unroll
      for (int w = 0; w < 4; ++w) s += *(LAS f32x4*)(red + (2 * w + wn) * RS + 8192 + lane * 16);
      *(f32x4*)((float*)(P.ws + WS_ZP) + (size_t)(st * 32 + head) * 64 + lane * 4) = s;
    }
  }
}

DI void o_unit(const Params& P, LAS char* lds, int st, int hp) {
  const int tid = threadIdx.x, lane = tid & 63;
  const int wid = __builtin_amdgcn_readfirstlane(tid >> 6);
  const int g = wid >> 2, w4 = wid & 3, wm2 = w4 >> 1, wn = w4 & 1;
  const int head = 2 * hp + wn, tokb = 256 * st + 128 * g + 64 * wm2;
  const int h2 = lane >> 5, r = lane & 31;
  const int ks = 2 * g + wm2, fb = ks >> 1, s = ks & 1;
  {
    const float* kvp = (const float*)(P.ws + WS_KVP) + (size_t)head * 2048 + (fb * 64 + lane) * 16 + 8 * s;
    const float* zp = (const float*)(P.ws + WS_ZP) + (size_t)head * 64 + (fb * 2 + h2) * 16 + 8 * s;
    f32x4 a0 = {0.f, 0.f, 0.f, 0.f}, a1 = a0, z0 = a0, z1 = a0;
#pragma unroll
    for (int t = 0; t < 8; ++t) {
      a0 += *(const f32x4*)(kvp + (size_t)t * 32 * 2048); a1 += *(const f32x4*)(kvp + (size_t)t * 32 * 2048 + 4);
      z0 += *(const f32x4*)(zp + (size_t)t * 32 * 64); z1 += *(const f32x4*)(zp + (size_t)t * 32 * 64 + 4);
    }
    *(LAS u32x4*)(lds + ((wn * 4 + ks) * 2) * 1024 + lane * 16) = (u32x4){pk2(a0[0], a0[1]), pk2(a0[2], a0[3]), pk2(a1[0], a1[1]), pk2(a1[2], a1[3])};
    *(LAS u32x4*)(lds + ((wn * 4 + ks) * 2 + 1) * 1024 + lane * 16) = (u32x4){pk2(z0[0], z0[1]), pk2(z0[2], z0[3]), pk2(z1[0], z1[1]), pk2(z1[2], z1[3])};
  }
  __syncthreads();
  bf16x8 kva[4], za[4];
#pragma unroll
  for (int k = 0; k < 4; ++k) { kva[k] = rd8(lds + ((wn * 4 + k) * 2) * 1024 + lane * 16); za[k] = rd8(lds + ((wn * 4 + k) * 2 + 1) * 1024 + lane * 16); }
  const char* phq = P.ws + WS_PHQ + ((size_t)((st * 16 + hp) * 8 + wid) * 8) * 1024 + lane * 16;
  f32x16 zero;
#pragma unroll
  for (int i = 0; i < 16; ++i) zero[i] = 0.f;
#pragma unroll
  for (int tb = 0; tb < 2; ++tb) {
    f32x16 num = zero, den = zero;
#pragma unroll
    for (int k = 0; k < 4; ++k) { const bf16x8 ph = *(const bf16x8*)(phq + (tb * 4 + k) * 1024); num = MFMA32(kva[k], ph, num); den = MFMA32(za[k], ph, den); }
    const int tok = tokb + 32 * tb + r;
    const float sc = ((const float*)(P.ws + WS_GATE))[tok] / (den[0] + 1e-6f);
    char* dst = P.ws + WS_OT + ((size_t)head * 128 + (tok >> 4)) * 1024 + ((tok & 15) + 16 * h2) * 16;
    *(u32x4*)(dst) = (u32x4){pk2(num[0] * sc, num[1] * sc), pk2(num[2] * sc, num[3] * sc), pk2(num[8] * sc, num[9] * sc), pk2(num[10] * sc, num[11] * sc)};
    *(u32x4*)(dst + 512) = (u32x4){pk2(num[4] * sc, num[5] * sc), pk2(num[6] * sc, num[7] * sc), pk2(num[12] * sc, num[13] * sc), pk2(num[14] * sc, num[15] * sc)};
  }
}

namespace fc { constexpr int SLOT = 16384, NS = 8, NSL = 72; }
template <bool WO> DI void fc_step(LAS char* sl, const unsigned (&offT)[4], unsigned offL, f32x4 (&acc)[4][4], bf16x8 (&tf)[4], bf16x8 (&lf)[4]) {
#pragma unroll
  for (int i = 0; i < 4; ++i) { tf[i] = cat8(trd(sl + offT[i]), trd(sl + offT[i] + 4096)); lf[i] = rd8(sl + offL + i * 1024); }
}
DI void fc_tile(const Params& P, LAS char* lds, int stl, int dt, int c, unsigned* xflag, unsigned epoch) {
  using namespace fc;
  const int tid = threadIdx.x, lane = tid & 63;
  const int wid = __builtin_amdgcn_readfirstlane(tid >> 6);
  const int g = wid >> 2, w4 = wid & 3, wm = w4 >> 1, wn = w4 & 1;
  const int NM = c ? 40 : 72, J0 = c ? 72 : 0;
  const int rr = 4 * w4 + (lane >> 4), cc = lane & 15;
  const unsigned swz = (((cc >> 1) ^ (rr & 7)) << 5) + ((cc & 1) << 4);
  const unsigned voffW2 = rr * 4096 + swz, voffWo = rr * 2048 + swz, voffL = lane * 16;
  const char* w2src = (const char*)P.w2 + stl * 256 + (size_t)J0 * 32 * 4096;
  const char* wosrc = (const char*)P.wo + dt * 256;
  const char* usrc = P.ws + WS_UT + (size_t)J0 * 65536 + (size_t)(dt * 8 + 2 * w4) * 1024;
  const char* osrc = P.ws + WS_OT + (size_t)(stl * 8 + 2 * w4) * 1024;
  auto issue = [&](int j) {
    const unsigned sb = (unsigned)(j & (NS - 1)) * SLOT;
    if (j < NM) {
      const char* s_ = w2src + (size_t)j * (32 * 4096);
      dma16<0, false>(voffW2, s_, sb + w4 * 1024); dma16<0, false>(voffW2, s_ + 16 * 4096, sb + (w4 + 4) * 1024);
      const char* l_ = usrc + (size_t)j * 65536;
      dma16<0, true>(voffL, l_, sb + 8192 + 2 * w4 * 1024); dma16<1024, true>(voffL, l_, sb + 8192 + 2 * w4 * 1024);
    } else {
      const int hh = j - NM;
      const char* s_ = wosrc + (size_t)hh * (32 * 2048);
      dma16<0, false>(voffWo, s_, sb + w4 * 1024); dma16<0, false>(voffWo, s_ + 16 * 2048, sb + (w4 + 4) * 1024);
      const char* l_ = osrc + (size_t)hh * 131072;
      dma16<0, true>(voffL, l_, sb + 8192 + 2 * w4 * 1024); dma16<1024, true>(voffL, l_, sb + 8192 + 2 * w4 * 1024);
    }
  };
  const int kg = lane >> 4, q = (lane & 15) >> 2, p = lane & 3, r7 = 4 * (kg & 1) + q;
  const unsigned base0 = (4 * kg + q) * 256 + (r7 << 5) + 8 * p;
  unsigned offTs[4], offTd[4];
#pragma unroll
  for (int i = 0; i < 4; ++i) { offTs[i] = base0 ^ ((4 * wn + i) << 5); offTd[i] = base0 ^ ((4 * wm + i) << 5); }
  const unsigned offLd = 8192 + 4 * wm * 1024 + lane * 16, offLs = 8192 + 4 * wn * 1024 + lane * 16;
  float og[4];
#pragma unroll
  for (int i = 0; i < 4; ++i) og[i] = 1.0f - ((const float*)(P.ws + WS_GATE))[128 * stl + 64 * wn + 16 * i + (lane & 15)];

  f32x4 acc[4][4];
#pragma unroll
  for (int i = 0; i < 4; ++i)
#pragma unroll
    for (int k = 0; k < 4; ++k) acc[i][k] = (f32x4){0.f, 0.f, 0.f, 0.f};

  issue(g); issue(g + 2); issue(g + 4);
  WAIT_V(8); BAR();
  if (g == 1) BAR();
  int j = g;
  for (; j < NM; j += 2) {
    LAS char* sl = lds + (j & (NS - 1)) * SLOT;
    bf16x8 sf[4], df[4];
#pragma unroll
    for (int i = 0; i < 4; ++i) { sf[i] = cat8(trd(sl + offTs[i]), trd(sl + offTs[i] + 4096)); df[i] = rd8(sl + offLd + i * 1024); }
    if (j + 6 < NSL) issue(j + 6);
    LBAR();
    __builtin_amdgcn_s_setprio(1);
#pragma unroll
    for (int i = 0; i < 4; ++i)
#pragma unroll
      for (int k = 0; k < 4; ++k) acc[i][k] = MFMA16(df[i], sf[k], acc[i][k]);
    __builtin_amdgcn_s_setprio(0);
    if (j + 6 < NSL) WAIT_V(8); else if (j + 4 < NSL) WAIT_V(4); else WAIT_V(0);
    BAR();
  }
#pragma unroll
  for (int i = 0; i < 4; ++i)
#pragma unroll
    for (int k = 0; k < 4; ++k) acc[i][k] *= og[k];
  for (; j < NSL; j += 2) {
    LAS char* sl = lds + (j & (NS - 1)) * SLOT;
    bf16x8 sf[4], df[4];
#pragma unroll
    for (int i = 0; i < 4; ++i) { df[i] = cat8(trd(sl + offTd[i]), trd(sl + offTd[i] + 4096)); sf[i] = rd8(sl + offLs + i * 1024); }
    if (j + 6 < NSL) issue(j + 6);
    LBAR();
    __builtin_amdgcn_s_setprio(1);
#pragma unroll
    for (int i = 0; i < 4; ++i)
#pragma unroll
      for (int k = 0; k < 4; ++k) acc[i][k] = MFMA16(df[i], sf[k], acc[i][k]);
    __builtin_amdgcn_s_setprio(0);
    if (j + 6 < NSL) WAIT_V(8); else if (j + 4 < NSL) WAIT_V(4); else WAIT_V(0);
    BAR();
  }
  if (g == 0) BAR();
  LAS char* X = lds;
#pragma unroll
  for (int i = 0; i < 4; ++i)
#pragma unroll
    for (int e = 0; e < 2; ++e) *(LAS f32x4*)(X + (wid * 8 + i * 2 + e) * 1024 + lane * 16) = sel4(g, acc[i][e], acc[i][2 + e]);
  LBAR();
  f32x4 keep[4], send[4];
#pragma unroll
  for (int i = 0; i < 4; ++i) {
    const f32x4 p0 = *(LAS f32x4*)(X + ((wid ^ 4) * 8 + i * 2 + 0) * 1024 + lane * 16), p1 = *(LAS f32x4*)(X + ((wid ^ 4) * 8 + i * 2 + 1) * 1024 + lane * 16);
    const f32x4 t0 = sel4(g, acc[i][2], acc[i][0]) + p0, t1 = sel4(g, acc[i][3], acc[i][1]) + p1;
    keep[i] = sel4(c, t1, t0); send[i] = sel4(c, t0, t1);
  }
  const int pair = stl * 8 + dt;
  {
    char* xb = P.ws + WS_XCH + (size_t)(pair * 2 + c) * 32768;
    const __amdgpu_buffer_rsrc_t rs = __builtin_amdgcn_make_buffer_rsrc(xb, 0, 32768, 0x00020000);
#pragma unroll
    for (int i = 0; i < 4; ++i) __builtin_amdgcn_raw_buffer_store_b128(__builtin_bit_cast(u32x4, send[i]), rs, (wid * 4 + i) * 1024 + lane * 16, 0, 16);
  }
  WAIT_V(0);
  BAR();
  GAS unsigned* fl = (GAS unsigned*)xflag;
  if (tid == 0) __hip_atomic_store(fl + (pair * 2 + c), epoch, __ATOMIC_RELAXED, __HIP_MEMORY_SCOPE_AGENT);
  if (wid == 0) {
    for (unsigned spins = 0; spins < (1u << 22); ++spins) {
      if ((unsigned)__builtin_amdgcn_readfirstlane(__hip_atomic_load(fl + (pair * 2 + (1 - c)), __ATOMIC_RELAXED, __HIP_MEMORY_SCOPE_AGENT)) == epoch) break;
      __builtin_amdgcn_s_sleep(1);
    }
  }
  BAR();
  {
    char* xb = P.ws + WS_XCH + (size_t)(pair * 2 + (1 - c)) * 32768;
    const __amdgpu_buffer_rsrc_t rs = __builtin_amdgcn_make_buffer_rsrc(xb, 0, 32768, 0x00020000);
    const int sblk = 2 * g + c;
    const int srow = 128 * stl + 64 * wn + 16 * sblk + (lane & 15);
    const float ogv = 1.0f - ((const float*)(P.ws + WS_GATE))[srow];
    const float bias = ogv * bf2f(P.b2[srow]);
    u16* orow = P.out + (size_t)srow * D + 128 * dt + 64 * wm + 4 * kg;
#pragma unroll
    for (int i = 0; i < 4; ++i) {
      const f32x4 o = __builtin_bit_cast(f32x4, __builtin_amdgcn_raw_buffer_load_b128(rs, (wid * 4 + i) * 1024 + lane * 16, 0, 16));
      const f32x4 v = keep[i] + o + bias;
      *(u32x2*)(orow + 16 * i) = (u32x2){pk2(v[0], v[1]), pk2(v[2], v[3])};
    }
  }
}

__global__ __launch_bounds__(512, 2) void k_ln(Params P) {
  extern __shared__ __attribute__((aligned(16))) char smem[];
  ln_unit(P, (LAS char*)smem, blockIdx.x);
}
__global__ __launch_bounds__(512, 2) void k_a(Params P) {
  extern __shared__ __attribute__((aligned(16))) char smem[];
  const int b = blockIdx.x, xcd = b & 7, loc = b >> 3, i = loc & 15;
  if (loc < 16) mlp1_tile(P, (LAS char*)smem, 4 * (xcd & 1) + (i & 3), 4 * (xcd >> 1) + (i >> 2));
  else qkv_tile(P, (LAS char*)smem, 4 * (xcd & 1) + (i & 3), 4 * (xcd >> 1) + (i >> 2));
}
__global__ __launch_bounds__(512, 2) void k_o(Params P) {
  extern __shared__ __attribute__((aligned(16))) char smem[];
  o_unit(P, (LAS char*)smem, blockIdx.x >> 4, blockIdx.x & 15);
}
__global__ __launch_bounds__(512, 2) void k_c(Params P) {
  extern __shared__ __attribute__((aligned(16))) char smem[];
  const int b = blockIdx.x, xcd = b & 7, loc = b >> 3, i = loc & 15;
  fc_tile(P, (LAS char*)smem, 4 * (xcd >> 1) + (i & 3), 4 * (xcd & 1) + (i >> 2), loc >> 4, (unsigned*)(P.ws + WS_FLAGS), 1u);
}

extern "C" void kernel_launch(void* const* d_in, const int* in_sizes, int n_in, void* d_out, int out_size, void* d_ws, size_t ws_size, hipStream_t stream) {
  Params P;
  P.x = (const u16*)d_in[0]; P.wqkv = (const u16*)d_in[1]; P.wphi = (const u16*)d_in[2]; P.wo = (const u16*)d_in[3];
  P.w1 = (const u16*)d_in[5]; P.b1 = (const u16*)d_in[6]; P.w2 = (const u16*)d_in[7]; P.b2 = (const u16*)d_in[8]; P.gw = (const u16*)d_in[9];
  P.ws = (char*)d_ws; P.out = (u16*)d_out;
  (void)hipFuncSetAttribute((const void*)k_a, hipFuncAttributeMaxDynamicSharedMemorySize, 152 * 1024);
  (void)hipFuncSetAttribute((const void*)k_c, hipFuncAttributeMaxDynamicSharedMemorySize, 152 * 1024);
  (void)hipMemsetAsync(d_ws, 0, 4096, stream);
  k_ln<<<128, 512, 40 * 1024, stream>>>(P);
  k_a<<<256, 512, 152 * 1024, stream>>>(P);
  k_o<<<128, 512, 16 * 1024, stream>>>(P);
  k_c<<<256, 512, 152 * 1024, stream>>>(P);
}
```

```cpp
#include <hip/hip_runtime.h>
#include <hip/hip_bf16.h>

typedef unsigned short u16;
typedef short bf16x8 __attribute__((ext_vector_type(8)));
typedef short s16x4 __attribute__((ext_vector_type(4)));
typedef float f32x2 __attribute__((ext_vector_type(2)));
typedef float f32x4 __attribute__((ext_vector_type(4)));
typedef float f32x16 __attribute__((ext_vector_type(16)));
typedef unsigned u32x4 __attribute__((ext_vector_type(4)));
typedef unsigned u32x2 __attribute__((ext_vector_type(2)));
typedef __bf16 bf16x2_t __attribute__((ext_vector_type(2)));
#define LAS __attribute__((address_space(3)))
#define GAS __attribute__((address_space(1)))
#define DI __device__ __forceinline__

constexpr int S = 2048, D = 1024, H = 32, F = 3584;

constexpr size_t WS_FLAGS = 0;
constexpr size_t WS_GATE  = 65536;
constexpr size_t WS_HT    = 1u << 20;
constexpr size_t WS_UT    = WS_HT + (4u << 20);
constexpr size_t WS_OT    = WS_UT + (8u << 20);
constexpr size_t WS_KVP   = WS_OT + (4u << 20);
constexpr size_t WS_ZP    = WS_KVP + (2u << 20);
constexpr size_t WS_XCH   = WS_ZP + (1u << 20);
constexpr size_t WS_DBG   = 64u << 20;
constexpr int FL_G = 0, FL_H = 64, FL_KV = 256, FL_O = 1024, FL_U = 1536, FL_X = 2048;
constexpr unsigned SPIN_MAX = 1u << 18;
constexpr int LDS_BYTES = 147456 + 1024;

struct Params {
  const u16 *x, *wqkv, *wphi, *wo, *w1, *b1, *w2, *b2, *gw;
  char* ws; u16* out;
};
#define STAMP(n) do {} while (0)

DI float bf2f(u16 b) { return __uint_as_float(((unsigned)b) << 16); }
DI float bflo(unsigned w) { return __uint_as_float(w << 16); }
DI float bfhi(unsigned w) { return __uint_as_float(w & 0xffff0000u); }
DI unsigned pk2(float a, float b) { bf16x2_t v = {(__bf16)a, (__bf16)b}; return __builtin_bit_cast(unsigned, v); }
DI bf16x8 mk8(unsigned a, unsigned b, unsigned c, unsigned d) { u32x4 v = {a, b, c, d}; return __builtin_bit_cast(bf16x8, v); }
DI bf16x8 cat8(s16x4 lo, s16x4 hi) { return __builtin_shufflevector(lo, hi, 0, 1, 2, 3, 4, 5, 6, 7); }

template <int IMM, bool SC1> DI void dma16(unsigned voff, const void* sbase, unsigned ldsaddr) {
  if (SC1) asm volatile("s_mov_b32 m0, %2\n\ts_nop 0\n\tglobal_load_lds_dwordx4 %0, %1 offset:%3 sc1" ::"v"(voff), "s"(sbase), "s"(ldsaddr), "i"(IMM) : "memory");
  else asm volatile("s_mov_b32 m0, %2\n\ts_nop 0\n\tglobal_load_lds_dwordx4 %0, %1 offset:%3" ::"v"(voff), "s"(sbase), "s"(ldsaddr), "i"(IMM) : "memory");
}
template <int N> DI void wait_v() { asm volatile("s_waitcnt vmcnt(%0)" ::"n"(N) : "memory"); }
template <int V> struct IC { static constexpr int value = V; };
#define SB() __builtin_amdgcn_sched_barrier(0)
#define WAIT_V(n) asm volatile("s_waitcnt vmcnt(" #n ")" ::: "memory")
#define WAIT_L0() asm volatile("s_waitcnt lgkmcnt(0)" ::: "memory")
#define BAR() do { asm volatile("s_barrier" ::: "memory"); SB(); } while (0)
#define LBAR() do { asm volatile("s_waitcnt lgkmcnt(0)\n\ts_barrier" ::: "memory"); SB(); } while (0)
DI s16x4 trd(LAS char* p) { return __builtin_amdgcn_ds_read_tr16_b64_v4i16((LAS s16x4*)p); }
DI bf16x8 rd8(LAS char* p) { return *(LAS bf16x8*)p; }
#define MFMA16(a, b, c) __builtin_amdgcn_mfma_f32_16x16x32_bf16((a), (b), (c), 0, 0, 0)
#define MFMA32(a, b, c) __builtin_amdgcn_mfma_f32_32x32x16_bf16((a), (b), (c), 0, 0, 0)

DI void mma16(f32x4& c, const bf16x8& a, const bf16x8& b) { c = MFMA16(a, b, c); }
DI void mma32(f32x16& c, const bf16x8& a, const bf16x8& b) { c = MFMA32(a, b, c); }
#define MFMA_DONE() SB()
DI int opaque(int n) { asm volatile("" : "+s"(n)); return n; }
template <int CTRL> DI float dpp_mov(float v) { return __builtin_bit_cast(float, __builtin_amdgcn_update_dpp(0, __builtin_bit_cast(int, v), CTRL, 0xF, 0xF, true)); }
DI f32x2 pair32(float v) { const unsigned u = __float_as_uint(v); auto r = __builtin_amdgcn_permlane32_swap(u, u, false, false); return (f32x2){__uint_as_float(r[0]), __uint_as_float(r[1])}; }
DI f32x2 pair16(float v) { const unsigned u = __float_as_uint(v); auto r = __builtin_amdgcn_permlane16_swap(u, u, false, false); return (f32x2){__uint_as_float(r[0]), __uint_as_float(r[1])}; }
DI float wave_sum(float v) {
  v += dpp_mov<0xB1>(v); v += dpp_mov<0x4E>(v); v += dpp_mov<0x141>(v); v += dpp_mov<0x140>(v);
  f32x2 a = pair16(v); v = a.x + a.y;
  f32x2 b = pair32(v); return b.x + b.y;
}

#define RLX_AGENT __ATOMIC_RELAXED, __HIP_MEMORY_SCOPE_AGENT
DI GAS unsigned* flagp(const Params& P, int w) { return (GAS unsigned*)(P.ws + WS_FLAGS) + w; }
DI void publish(const Params& P, int w, unsigned nonce) {
  WAIT_V(0); BAR();
  if (threadIdx.x == 0) __hip_atomic_store(flagp(P, w), nonce, RLX_AGENT);
}
DI void poll_n(const Params& P, int w0, int n, unsigned nonce) {
  const int lane = threadIdx.x & 63;
  GAS unsigned* p0 = flagp(P, w0 + (lane < n ? lane : 0));
  GAS unsigned* p1 = flagp(P, w0 + (lane + 64 < n ? lane + 64 : 0));
  for (unsigned spins = 0; spins < SPIN_MAX; ++spins) {
    const unsigned a = __hip_atomic_load(p0, RLX_AGENT), b = __hip_atomic_load(p1, RLX_AGENT);
    if (__all(a == nonce && b == nonce)) break;
    __builtin_amdgcn_s_sleep(1);
  }
}
DI float ld_gate(const Params& P, int tok) { return __uint_as_float(__hip_atomic_load((GAS unsigned*)(P.ws + WS_GATE) + tok, RLX_AGENT)); }
DI __amdgpu_buffer_rsrc_t rsrc(const void* p, unsigned bytes) { return __builtin_amdgcn_make_buffer_rsrc((void*)p, 0, bytes, 0x00020000); }
DI void st16(__amdgpu_buffer_rsrc_t r, unsigned off, u32x4 v) { __builtin_amdgcn_raw_buffer_store_b128(v, r, off, 0, 16); }
DI f32x4 ld16(__amdgpu_buffer_rsrc_t r, unsigned off) { return __builtin_bit_cast(f32x4, __builtin_amdgcn_raw_buffer_load_b128(r, off, 0, 16)); }

DI void ln_unit(const Params& P, LAS char* lds, int m) {
  const int tid = threadIdx.x, lane = tid & 63, w = tid >> 6;
  const int kb = m >> 1, hf = m & 1;
  auto xrow = [&](int i) { return lds + (i >> 2) * 24576 + 16384 + (i & 3) * 2048; };
  LAS f32x2* st = (LAS f32x2*)(lds + 147456);
  u32x4 v[2][2];
#pragma unroll
  for (int e = 0; e < 2; ++e) {
    const int i = 2 * w + e, tok = 32 * kb + (i < 8 ? 8 * hf + i : 16 + 8 * hf + (i - 8));
    v[e][0] = *(const u32x4*)(P.x + (size_t)tok * D + lane * 8);
    v[e][1] = *(const u32x4*)(P.x + (size_t)tok * D + 512 + lane * 8);
  }
  const u32x4 g0 = *(const u32x4*)(P.gw + lane * 8), g1 = *(const u32x4*)(P.gw + 512 + lane * 8);
#pragma unroll
  for (int e = 0; e < 2; ++e) {
    const int i = 2 * w + e, tok = 32 * kb + (i < 8 ? 8 * hf + i : 16 + 8 * hf + (i - 8));
    float s = 0.f, q = 0.f, gd = 0.f;
#pragma unroll
    for (int c = 0; c < 2; ++c)
#pragma unroll
      for (int j = 0; j < 4; ++j) {
        const unsigned xw = v[e][c][j], gw = c ? g1[j] : g0[j];
        const float a = bflo(xw), b = bfhi(xw);
        s += a + b; q += a * a + b * b; gd += a * bflo(gw) + b * bfhi(gw);
      }
    s = wave_sum(s); q = wave_sum(q); gd = wave_sum(gd);
    const float mean = s * (1.0f / D);
    const float var = fmaxf(q * (1.0f / D) - mean * mean, 0.f);
    const float rstd = 1.0f / sqrtf(var + 1e-5f);
    *(LAS u32x4*)(xrow(i) + lane * 16) = v[e][0];
    *(LAS u32x4*)(xrow(i) + 1024 + lane * 16) = v[e][1];
    if (lane == 0) {
      st[i] = (f32x2){mean, rstd};
      __hip_atomic_store((GAS unsigned*)(P.ws + WS_GATE) + tok, __float_as_uint(1.0f / (1.0f + __expf(-gd))), RLX_AGENT);
    }
  }
  LBAR();
  float h0[16], h1[16];
#pragma unroll
  for (int i = 0; i < 16; ++i) {
    const unsigned xw = *(LAS unsigned*)(xrow(i) + tid * 4); const f32x2 s = st[i];
    h0[i] = (bflo(xw) - s.x) * s.y; h1[i] = (bfhi(xw) - s.x) * s.y;
  }
  const int d = 2 * tid;
  const __amdgpu_buffer_rsrc_t rs = rsrc(P.ws + WS_HT, 4u << 20);
  const unsigned dst = ((kb * 64 + (d >> 4)) * 64 + (d & 15)) * 16;
#pragma unroll
  for (int e = 0; e < 2; ++e) {
    const int kg = 2 * hf + e, a = 4 * e, b = 8 + 4 * e;
    st16(rs, dst + kg * 256, (u32x4){pk2(h0[a], h0[a + 1]), pk2(h0[a + 2], h0[a + 3]), pk2(h0[b], h0[b + 1]), pk2(h0[b + 2], h0[b + 3])});
    st16(rs, dst + kg * 256 + 16, (u32x4){pk2(h1[a], h1[a + 1]), pk2(h1[a + 2], h1[a + 3]), pk2(h1[b], h1[b + 1]), pk2(h1[b + 2], h1[b + 3])});
  }
}

namespace fc { constexpr int SLOT = 16384, NS = 8; }
struct FcId { int stl, dt, c; };
DI void fc_prologue_w(const Params& P, const FcId id) {
  using namespace fc;
  const int lane = threadIdx.x & 63, wid = __builtin_amdgcn_readfirstlane(threadIdx.x >> 6), g = wid >> 2, w4 = wid & 3;
  const int rr = 4 * w4 + (lane >> 4), cc = lane & 15;
  const unsigned voffW2 = rr * 4096 + (((cc >> 1) ^ (rr & 7)) << 5) + ((cc & 1) << 4);
  const char* src = (const char*)P.w2 + id.stl * 256 + (size_t)((id.c ? 72 : 0) + g) * 32 * 4096;
#pragma unroll
  for (int r = 0; r < 4; ++r) {
    const unsigned sb = (g + 2 * r) * SLOT;
    dma16<0, false>(voffW2, src + (size_t)r * (64 * 4096), sb + w4 * 1024);
    dma16<0, false>(voffW2, src + (size_t)r * (64 * 4096) + 16 * 4096, sb + (w4 + 4) * 1024);
  }
}

namespace m1 { constexpr int SLOT = 24576, NS = 6; }
DI f32x2 gelu2(f32x2 x) {
  constexpr float C0 = -2.885390081777927f * 0.7978845608028654f, C1 = C0 * 0.044715f;
  const f32x2 t = x * x * C1 + C0;
  const f32x2 a = x * t;
  f32x2 e = {__builtin_amdgcn_exp2f(a.x), __builtin_amdgcn_exp2f(a.y)};
  e = e + 1.0f;
  const f32x2 r = {__builtin_amdgcn_rcpf(e.x), __builtin_amdgcn_rcpf(e.y)};
  return x * r;
}
DI void mlp1_role(const Params& P, LAS char* lds, int m, int dt, int ft, const FcId fid, unsigned nonce) {
  using namespace m1;
  const int tid = threadIdx.x, lane = tid & 63;
  const int wid = __builtin_amdgcn_readfirstlane(tid >> 6);
  const int g = wid >> 2, w4 = wid & 3, wm = w4 >> 1, wn = w4 & 1, odd = ft & 1;
  const char* wptr = (const char*)P.w1 + (224 * ft - 32 * odd) * 2 + (size_t)g * (32 * 7168);
  const char* hptr = P.ws + WS_HT + (size_t)(dt * 8 + 2 * w4) * 1024 + (size_t)g * 65536;
  const int r0 = 2 * w4 + (lane >> 5), c = lane & 31;
  const unsigned voffW = r0 * 7168 + (((c >> 1) ^ r0) << 5) + ((c & 1) << 4);
  const unsigned voffH = lane * 16;
  STAMP(0);
#pragma unroll
  for (int r = 0; r < 3; ++r)
#pragma unroll
    for (int i = 0; i < 4; ++i) dma16<0, false>(voffW, wptr + (size_t)r * (64 * 7168) + i * (8 * 7168), (g + 2 * r) * SLOT + (w4 + 4 * i) * 1024);
  ln_unit(P, lds, m);
  STAMP(1);
  publish(P, FL_H + m, nonce);
  if (wid == 0) poll_n(P, FL_H, 128, nonce);
  BAR();
  STAMP(2);
#pragma unroll
  for (int r = 0; r < 3; ++r) {
    dma16<0, true>(voffH, hptr + (size_t)r * (2 * 65536), (g + 2 * r) * SLOT + 16384 + 2 * w4 * 1024);
    dma16<1024, true>(voffH, hptr + (size_t)r * (2 * 65536), (g + 2 * r) * SLOT + 16384 + 2 * w4 * 1024);
  }
  wptr += 3 * 64 * 7168; hptr += 3 * 2 * 65536;
  unsigned sbi = g * SLOT;
  auto issue1 = [&](int i) {
    if (i < 4) dma16<0, false>(voffW, wptr + i * (8 * 7168), sbi + (w4 + 4 * i) * 1024);
    else if (i == 4) dma16<0, true>(voffH, hptr, sbi + 16384 + 2 * w4 * 1024);
    else {
      dma16<1024, true>(voffH, hptr, sbi + 16384 + 2 * w4 * 1024);
      wptr += 2 * 32 * 7168; hptr += 2 * 65536;
      sbi += 2 * SLOT; sbi = sbi >= NS * SLOT ? sbi - NS * SLOT : sbi;
    }
  };
  const int kg = lane >> 4, q = (lane & 15) >> 2, p = lane & 3, r7 = 4 * (kg & 1) + q;
  const unsigned base0 = (4 * kg + q) * 512 + (r7 << 5) + 8 * p;
  const int u0 = 7 * wn + 2 * odd;
  unsigned offW[7];
#pragma unroll
  for (int i = 0; i < 7; ++i) offW[i] = base0 ^ ((u0 + i) << 5);
  const unsigned offH = 16384 + 4 * wm * 1024 + lane * 16;

  f32x4 acc[7][4];
#pragma unroll
  for (int i = 0; i < 7; ++i)
#pragma unroll
    for (int d = 0; d < 4; ++d) acc[i][d] = (f32x4){0.f, 0.f, 0.f, 0.f};

  WAIT_V(0); BAR();
  STAMP(3);
  unsigned sbr = g * SLOT;
  bf16x8 wA[7], hA[4], wB[7], hB[4];
  auto rd1 = [&](int i, LAS char* sl, bf16x8 (&wf)[7], bf16x8 (&hf)[4]) {
    if (i < 4) hf[i] = rd8(sl + offH + i * 1024);
    else wf[i - 4] = cat8(trd(sl + offW[i - 4]), trd(sl + offW[i - 4] + 8192));
  };
  {
    LAS char* sl = lds + sbr;
#pragma unroll
    for (int i = 0; i < 11; ++i) rd1(i, sl, wA, hA);
    sbr += 2 * SLOT; sbr = sbr >= NS * SLOT ? sbr - NS * SLOT : sbr;
  }
  auto step = [&](auto READ, auto ISSUE, auto WAITN, bf16x8 (&wc)[7], bf16x8 (&hc)[4], bf16x8 (&wx)[7], bf16x8 (&hx)[4]) {
    if constexpr (WAITN.value >= 0) wait_v<WAITN.value>();
    LBAR();
    LAS char* sl = lds + sbr;
#pragma unroll
    for (int mm = 0; mm < 28; ++mm) {
      mma16(acc[mm >> 2][mm & 3], wc[mm >> 2], hc[mm & 3]);
      SB();
      if constexpr (READ.value) { if (mm < 11) rd1(mm, sl, wx, hx); }
      if constexpr (ISSUE.value) { if (mm >= 12 && mm < 24 && (mm & 1) == 0) issue1((mm - 12) >> 1); }
      SB();
    }
    sbr += 2 * SLOT; sbr = sbr >= NS * SLOT ? sbr - NS * SLOT : sbr;
  };
  for (int it = 0; it < 14; ++it) { step(IC<1>{}, IC<1>{}, IC<6>{}, wA, hA, wB, hB); step(IC<1>{}, IC<1>{}, IC<6>{}, wB, hB, wA, hA); }
  for (int it = opaque(1); it > 0; --it) { step(IC<1>{}, IC<1>{}, IC<6>{}, wA, hA, wB, hB); step(IC<1>{}, IC<0>{}, IC<6>{}, wB, hB, wA, hA); }
  for (int it = opaque(1); it > 0; --it) { step(IC<1>{}, IC<0>{}, IC<0>{}, wA, hA, wB, hB); step(IC<0>{}, IC<0>{}, IC<-1>{}, wB, hB, wA, hA); }
  MFMA_DONE();
  LBAR();
  STAMP(4);
  auto xaddr = [&](int w, int e) { return lds + (w * 15 + e) * 1024 + lane * 16; };
  const int wP = wid ^ 4, wQ0 = wid ^ 1, wQ1 = wid ^ 5;
  const __amdgpu_buffer_rsrc_t urs = rsrc(P.ws + WS_UT, 8u << 20);
  const unsigned utile = (7 * ft) * 65536 + (dt * 8 + 4 * wm + 2 * g) * 1024 + lane * 16;
  const u16* b1p = P.b1 + 224 * ft + 4 * kg;
  auto epi = [&](auto G, auto WN) {
    constexpr int gg = G.value, wnn = WN.value, IM = wnn ? 0 : 6, IMQ = wnn ? 6 : 0;
    u32x2 bias[7];
#pragma unroll
    for (int i = 0; i < 7; ++i) bias[i] = *(const u32x2*)(b1p + 16 * (7 * wnn + i));
    const u32x2 biasq = *(const u32x2*)(b1p + 16 * (wnn ? 6 : 7));
#pragma unroll
    for (int dd = 0; dd < 2; ++dd)
#pragma unroll
      for (int i = 0; i < 7; ++i) *(LAS f32x4*)xaddr(wid, dd * 7 + i) = acc[i][2 * (1 - gg) + dd];
    *(LAS f32x4*)xaddr(wid, 14) = acc[IM][2 * gg + (1 - wnn)];
    LBAR();
    f32x4 t[2][7], tq;
#pragma unroll
    for (int dd = 0; dd < 2; ++dd)
#pragma unroll
      for (int i = 0; i < 7; ++i)
        if (i != IM || dd == wnn) t[dd][i] = acc[i][2 * gg + dd] + *(LAS f32x4*)xaddr(wP, dd * 7 + i);
    tq = *(LAS f32x4*)xaddr(wQ0, 14) + *(LAS f32x4*)xaddr(wQ1, wnn * 7 + IMQ);
    LBAR();
    asm volatile("" : "+v"(bias[0]), "+v"(bias[1]), "+v"(bias[2]), "+v"(bias[3]), "+v"(bias[4]), "+v"(bias[5]), "+v"(bias[6]));
    u32x2 bq = biasq; asm volatile("" : "+v"(bq));
    fc_prologue_w(P, fid);
    auto fin = [&](f32x4 v, u32x2 bw) -> u32x2 {
      const f32x2 a = gelu2((f32x2){v[0] + bflo(bw[0]), v[1] + bfhi(bw[0])}), b = gelu2((f32x2){v[2] + bflo(bw[1]), v[3] + bfhi(bw[1])});
      return (u32x2){pk2(a.x, a.y), pk2(b.x, b.y)};
    };
#pragma unroll
    for (int dd = 0; dd < 2; ++dd)
#pragma unroll
      for (int pr = 0; pr < 3; ++pr) {
        const int i0 = 2 * pr + wnn, b = 7 * wnn + i0;
        const u32x2 lo = fin(t[dd][i0], bias[i0]), hi = fin(t[dd][i0 + 1], bias[i0 + 1]);
        st16(urs, utile + (b >> 1) * 65536 + dd * 1024, (u32x4){lo[0], lo[1], hi[0], hi[1]});
      }
    {
      const u32x2 mine = fin(t[wnn][IM], bias[IM]), oth = fin(tq, bq);
      st16(urs, utile + 3 * 65536 + wnn * 1024, wnn ? (u32x4){oth[0], oth[1], mine[0], mine[1]} : (u32x4){mine[0], mine[1], oth[0], oth[1]});
    }
  };
  if (g == 0) { if (wn == 0) epi(IC<0>{}, IC<0>{}); else epi(IC<0>{}, IC<1>{}); }
  else { if (wn == 0) epi(IC<1>{}, IC<0>{}); else epi(IC<1>{}, IC<1>{}); }
  STAMP(5);
  publish(P, FL_U + dt * 32 + ft, nonce);
  STAMP(6);
}

namespace qk { constexpr int SLOT = 57344, WOFF = 32768, NT = 16; }
DI void softmax_pm(const f32x16& pq, u32x4 (&out)[4]) {
  float mx = fmaxf(pq[0], pq[1]), mn = fminf(pq[0], pq[1]);
#pragma unroll
  for (int i = 2; i < 16; i += 2) { mx = fmaxf(fmaxf(mx, pq[i]), pq[i + 1]); mn = fminf(fminf(mn, pq[i]), pq[i + 1]); }
  { const f32x2 a = pair32(mx), b = pair32(mn); mx = fmaxf(a.x, a.y); mn = fminf(b.x, b.y); }
  constexpr float L2E = 1.4426950408889634f;
  const float mxs = -mx * L2E, mns = mn * L2E;
  f32x2 ep[8], en[8], sp = {0.f, 0.f}, sn = {0.f, 0.f};
#pragma unroll
  for (int i = 0; i < 8; ++i) {
    const f32x2 pv = {pq[2 * i], pq[2 * i + 1]};
    const f32x2 a = pv * L2E + mxs, b = mns - pv * L2E;
    ep[i] = (f32x2){__builtin_amdgcn_exp2f(a.x), __builtin_amdgcn_exp2f(a.y)};
    en[i] = (f32x2){__builtin_amdgcn_exp2f(b.x), __builtin_amdgcn_exp2f(b.y)};
    sp += ep[i]; sn += en[i];
  }
  const f32x2 c = pair32(sp.x + sp.y), d = pair32(sn.x + sn.y);
  const float rp = __builtin_amdgcn_rcpf(c.x + c.y), rn = __builtin_amdgcn_rcpf(d.x + d.y);
#pragma unroll
  for (int s = 0; s < 2; ++s)
#pragma unroll
    for (int j = 0; j < 4; ++j) {
      const f32x2 a = ep[4 * s + j] * rp, b = en[4 * s + j] * rn;
      out[s][j] = pk2(a.x, a.y); out[2 + s][j] = pk2(b.x, b.y);
    }
}
DI bf16x8 pack8(const f32x16& x, int s) {
  return mk8(pk2(x[8 * s], x[8 * s + 1]), pk2(x[8 * s + 2], x[8 * s + 3]), pk2(x[8 * s + 4], x[8 * s + 5]), pk2(x[8 * s + 6], x[8 * s + 7]));
}
DI void qkv_role(const Params& P, LAS char* lds, int st, int hp, const FcId fid, unsigned nonce) {
  using namespace qk;
  const int tid = threadIdx.x, lane = tid & 63;
  const int wid = __builtin_amdgcn_readfirstlane(tid >> 6);
  const int g = wid >> 2, w4 = wid & 3, wm2 = w4 >> 1, wn = w4 & 1;
  const int head = 2 * hp + wn, tokb = 128 * g + 64 * wm2;
  const int h2 = lane >> 5, r = lane & 31;
  unsigned voffW[3];
#pragma unroll
  for (int b = 0; b < 3; ++b) {
    const int li = 8 * b + (lane >> 3), row = li / 3, part = li % 3;
    voffW[b] = row * 6144 + part * 2048 + (((lane & 7) ^ (((row >> 1) & 1) << 2)) << 4);
  }
  const unsigned voffX = (lane >> 3) * 2048 + (((lane & 7) ^ ((4 * (wid & 1) + (lane >> 4)) & 7)) << 4);
  const char* wsrc = (const char*)P.wqkv + hp * 128;
  const char* xsrc = (const char*)P.x + (size_t)(256 * st) * 2048;
  auto issue1 = [&](int t, int i) {
    const unsigned sb = (t & 1) * SLOT;
    if (i < 3) dma16<0, false>(voffW[i], wsrc + (size_t)(64 * t + 8 * wid) * 6144, sb + WOFF + (3 * wid + i) * 1024);
    else dma16<0, false>(voffX, xsrc + (size_t)(wid + 8 * (i - 3)) * 16384 + t * 128, sb + (wid + 8 * (i - 3)) * 1024);
  };
  STAMP(0);
#pragma unroll
  for (int i = 0; i < 7; ++i) issue1(0, i);
#pragma unroll
  for (int i = 0; i < 7; ++i) issue1(1, i);
  bf16x8 wphi[2];
#pragma unroll
  for (int s = 0; s < 2; ++s)
#pragma unroll
    for (int j = 0; j < 8; ++j) wphi[s][j] = (short)P.wphi[(head * 32 + 16 * s + 8 * (j >> 2) + 4 * h2 + (j & 3)) * 32 + r];
  asm volatile("" : "+v"(wphi[0]), "+v"(wphi[1]));
  unsigned offX[4];
#pragma unroll
  for (int s = 0; s < 4; ++s) offX[s] = (tokb + r) * 128 + (((2 * s + h2) ^ ((r >> 1) & 7)) << 4);
  const int blk = (lane >> 4) & 1, q = (lane & 15) >> 2, p = lane & 3;
  const unsigned offW = WOFF + (8 * h2 + q) * 384 + 64 * (wn ^ (q >> 1)) + 32 * blk + 8 * p;

  f32x16 aq[2], ak[2], av[2];
#pragma unroll
  for (int t = 0; t < 2; ++t)
#pragma unroll
    for (int i = 0; i < 16; ++i) { aq[t][i] = 0.f; ak[t][i] = 0.f; av[t][i] = 0.f; }

  WAIT_V(0); BAR();
  STAMP(3);
  bf16x8 xA[2][2], wA[3][2], xB[2][2], wB[3][2];
  auto rd1 = [&](int i, LAS char* sl, int hf, bf16x8 (&xf)[2][2], bf16x8 (&wf)[3][2]) {
    if (i < 4) xf[i & 1][i >> 1] = rd8(sl + offX[2 * hf + (i >> 1)] + (i & 1) * 4096);
    else { const int part = (i - 4) % 3, s2 = (i - 4) / 3, s = 2 * hf + s2; wf[part][s2] = cat8(trd(sl + offW + (16 * s) * 384 + part * 128), trd(sl + offW + (16 * s + 4) * 384 + part * 128)); }
  };
#pragma unroll
  for (int i = 0; i < 10; ++i) rd1(i, lds, 0, xA, wA);
  auto half = [&](auto READ, auto ISSUE, LAS char* sl, int hf, int tn, bf16x8 (&xc)[2][2], bf16x8 (&wc)[3][2], bf16x8 (&xx)[2][2], bf16x8 (&wx)[3][2]) {
#pragma unroll
    for (int m = 0; m < 12; ++m) {
      const int s2 = m / 6, tb = (m % 6) / 3, part = m % 3;
      if (part == 0) mma32(aq[tb], wc[0][s2], xc[tb][s2]);
      else if (part == 1) mma32(ak[tb], wc[1][s2], xc[tb][s2]);
      else mma32(av[tb], xc[tb][s2], wc[2][s2]);
      SB();
      if constexpr (READ.value) { if (m < 10) rd1(m, sl, hf, xx, wx); }
      if constexpr (ISSUE.value) { if (m < 7) issue1(tn, m); }
      SB();
    }
  };
  for (int t = 0; t < NT - 2; ++t) {
    LAS char* s0 = lds + (t & 1) * SLOT; LAS char* s1 = lds + ((t + 1) & 1) * SLOT;
    half(IC<1>{}, IC<0>{}, s0, 1, 0, xA, wA, xB, wB);
    WAIT_V(0); LBAR();
    half(IC<1>{}, IC<1>{}, s1, 0, t + 2, xB, wB, xA, wA);
  }
  for (int it = opaque(1); it > 0; --it) {
    half(IC<1>{}, IC<0>{}, lds, 1, 0, xA, wA, xB, wB);
    WAIT_V(0); LBAR();
    half(IC<1>{}, IC<0>{}, lds + SLOT, 0, 0, xB, wB, xA, wA);
  }
  for (int it = opaque(1); it > 0; --it) {
    half(IC<1>{}, IC<0>{}, lds + SLOT, 1, 0, xA, wA, xB, wB);
    half(IC<0>{}, IC<0>{}, lds, 0, 0, xB, wB, xA, wA);
  }
  MFMA_DONE();
  LBAR();
  STAMP(4);
  LAS char* img = lds + wid * 8192;
  f32x16 zero;
#pragma unroll
  for (int i = 0; i < 16; ++i) zero[i] = 0.f;
  const unsigned sw = (r & 1) | (((r >> 2) & 3) << 1);
#pragma unroll
  for (int tb = 0; tb < 2; ++tb) {
    f32x16 pk = MFMA32(wphi[0], pack8(ak[tb], 0), zero); pk = MFMA32(wphi[1], pack8(ak[tb], 1), pk);
    u32x4 ph[4]; softmax_pm(pk, ph);
#pragma unroll
    for (int fb = 0; fb < 2; ++fb)
#pragma unroll
      for (int g4 = 0; g4 < 4; ++g4) {
        const u32x2 w = {ph[2 * fb + (g4 >> 1)][2 * (g4 & 1)], ph[2 * fb + (g4 >> 1)][2 * (g4 & 1) + 1]};
        *(LAS u32x2*)(img + (32 * tb + r) * 128 + 64 * (fb ^ ((r >> 1) & 1)) + 8 * ((2 * g4 + h2) ^ sw)) = w;
      }
  }
  WAIT_L0();
  f32x16 kv[2] = {zero, zero}, zz[2] = {zero, zero};
  const bf16x8 ones = mk8(0x3f803f80u, 0x3f803f80u, 0x3f803f80u, 0x3f803f80u);
#pragma unroll
  for (int tb = 0; tb < 2; ++tb)
#pragma unroll
    for (int s = 0; s < 2; ++s) {
      const bf16x8 vb = pack8(av[tb], s);
#pragma unroll
      for (int fb = 0; fb < 2; ++fb) {
        s16x4 a[2];
#pragma unroll
        for (int t = 0; t < 2; ++t) {
          const int row = 16 * s + 8 * t + 4 * h2 + q;
          const unsigned swr = (row & 1) | (((row >> 2) & 3) << 1);
          a[t] = trd(img + (32 * tb + row) * 128 + 64 * (fb ^ ((row >> 1) & 1)) + 8 * ((4 * blk + p) ^ swr));
        }
        const bf16x8 af = cat8(a[0], a[1]);
        kv[fb] = MFMA32(af, vb, kv[fb]); zz[fb] = MFMA32(af, ones, zz[fb]);
      }
    }
  LAS char* red = lds + 65536;
  constexpr int RS = 8448;
#pragma unroll
  for (int fb = 0; fb < 2; ++fb) {
#pragma unroll
    for (int c = 0; c < 4; ++c) *(LAS f32x4*)(red + wid * RS + (fb * 4 + c) * 1024 + lane * 16) = (f32x4){kv[fb][4 * c], kv[fb][4 * c + 1], kv[fb][4 * c + 2], kv[fb][4 * c + 3]};
    if (r == 0) {
#pragma unroll
      for (int c = 0; c < 4; ++c) *(LAS f32x4*)(red + wid * RS + 8192 + (fb * 2 + h2) * 64 + c * 16) = (f32x4){zz[fb][4 * c], zz[fb][4 * c + 1], zz[fb][4 * c + 2], zz[fb][4 * c + 3]};
    }
  }
  LBAR();
  const int qd = 2 * g + wm2;
  const __amdgpu_buffer_rsrc_t kvrs = rsrc(P.ws + WS_KVP, 2u << 20), zrs = rsrc(P.ws + WS_ZP, 65536);
  {
#pragma unroll
    for (int e = 0; e < 2; ++e) {
      const int ch = 2 * qd + e;
      f32x4 s = (f32x4){0.f, 0.f, 0.f, 0.f};
#pragma unroll
      for (int w = 0; w < 4; ++w) s += *(LAS f32x4*)(red + (2 * w + wn) * RS + ch * 1024 + lane * 16);
      st16(kvrs, ((st * 32 + head) * 2048 + ((ch >> 2) * 64 + lane) * 16 + 4 * (ch & 3)) * 4, __builtin_bit_cast(u32x4, s));
    }
    if (qd == 0 && lane < 16) {
      f32x4 s = (f32x4){0.f, 0.f, 0.f, 0.f};
#pragma unroll
      for (int w = 0; w < 4; ++w) s += *(LAS f32x4*)(red + (2 * w + wn) * RS + 8192 + lane * 16);
      st16(zrs, ((st * 32 + head) * 64 + lane * 4) * 4, __builtin_bit_cast(u32x4, s));
    }
  }
  WAIT_L0();
  publish(P, FL_KV + hp * 32 + st, nonce);
  STAMP(5);
  fc_prologue_w(P, fid);
  u32x4 phq[2][4];
#pragma unroll
  for (int tb = 0; tb < 2; ++tb) {
    f32x16 pq = MFMA32(wphi[0], pack8(aq[tb], 0), zero); pq = MFMA32(wphi[1], pack8(aq[tb], 1), pq);
    softmax_pm(pq, phq[tb]);
  }
  STAMP(6);
  if (wid == 0) { poll_n(P, FL_KV + hp * 32, 8, nonce); poll_n(P, FL_H, 128, nonce); }
  BAR();
  STAMP(7);
  LAS char* osc = lds + 131072;
  {
    const int ks = qd, fb = ks >> 1, s = ks & 1;
    const unsigned ko = (head * 2048 + (fb * 64 + lane) * 16 + 8 * s) * 4, zo = (head * 64 + (fb * 2 + h2) * 16 + 8 * s) * 4;
    f32x4 a0[8], a1[8], z0[8], z1[8];
#pragma unroll
    for (int t = 0; t < 8; ++t) { a0[t] = ld16(kvrs, ko + t * (32 * 8192)); a1[t] = ld16(kvrs, ko + t * (32 * 8192) + 16); z0[t] = ld16(zrs, zo + t * (32 * 256)); z1[t] = ld16(zrs, zo + t * (32 * 256) + 16); }
    const f32x4 sa0 = ((a0[0] + a0[1]) + (a0[2] + a0[3])) + ((a0[4] + a0[5]) + (a0[6] + a0[7])), sa1 = ((a1[0] + a1[1]) + (a1[2] + a1[3])) + ((a1[4] + a1[5]) + (a1[6] + a1[7]));
    const f32x4 sz0 = ((z0[0] + z0[1]) + (z0[2] + z0[3])) + ((z0[4] + z0[5]) + (z0[6] + z0[7])), sz1 = ((z1[0] + z1[1]) + (z1[2] + z1[3])) + ((z1[4] + z1[5]) + (z1[6] + z1[7]));
    *(LAS u32x4*)(osc + ((wn * 4 + ks) * 2) * 1024 + lane * 16) = (u32x4){pk2(sa0[0], sa0[1]), pk2(sa0[2], sa0[3]), pk2(sa1[0], sa1[1]), pk2(sa1[2], sa1[3])};
    *(LAS u32x4*)(osc + ((wn * 4 + ks) * 2 + 1) * 1024 + lane * 16) = (u32x4){pk2(sz0[0], sz0[1]), pk2(sz0[2], sz0[3]), pk2(sz1[0], sz1[1]), pk2(sz1[2], sz1[3])};
  }
  float gt[2];
#pragma unroll
  for (int tb = 0; tb < 2; ++tb) gt[tb] = ld_gate(P, 256 * st + tokb + 32 * tb + r);
  LBAR();
  {
    bf16x8 kva[4], za[4];
#pragma unroll
    for (int k = 0; k < 4; ++k) { kva[k] = rd8(osc + ((wn * 4 + k) * 2) * 1024 + lane * 16); za[k] = rd8(osc + ((wn * 4 + k) * 2 + 1) * 1024 + lane * 16); }
    const __amdgpu_buffer_rsrc_t ors = rsrc(P.ws + WS_OT, 4u << 20);
#pragma unroll
    for (int tb = 0; tb < 2; ++tb) {
      f32x16 num = zero, den = zero;
#pragma unroll
      for (int k = 0; k < 4; ++k) { const bf16x8 ph = __builtin_bit_cast(bf16x8, phq[tb][k]); num = MFMA32(kva[k], ph, num); den = MFMA32(za[k], ph, den); }
      const int tok = 256 * st + tokb + 32 * tb + r;
      const float sc = gt[tb] * __builtin_amdgcn_rcpf(den[0] + 1e-6f);
      const unsigned dst = (head * 128 + (tok >> 4)) * 1024 + ((tok & 15) + 16 * h2) * 16;
      st16(ors, dst, (u32x4){pk2(num[0] * sc, num[1] * sc), pk2(num[2] * sc, num[3] * sc), pk2(num[8] * sc, num[9] * sc), pk2(num[10] * sc, num[11] * sc)});
      st16(ors, dst + 512, (u32x4){pk2(num[4] * sc, num[5] * sc), pk2(num[6] * sc, num[7] * sc), pk2(num[12] * sc, num[13] * sc), pk2(num[14] * sc, num[15] * sc)});
    }
  }
  publish(P, FL_O + st * 32 + hp, nonce);
  STAMP(8);
}

DI void fc_role(const Params& P, LAS char* lds, const FcId id, unsigned nonce) {
  using namespace fc;
  const int stl = id.stl, dt = id.dt, c = id.c;
  const int tid = threadIdx.x, lane = tid & 63;
  const int wid = __builtin_amdgcn_readfirstlane(tid >> 6);
  const int g = wid >> 2, w4 = wid & 3, wm = w4 >> 1, wn = w4 & 1;
  const int J0 = c ? 72 : 0;
  const int rr = 4 * w4 + (lane >> 4), cc = lane & 15;
  const unsigned swz = (((cc >> 1) ^ (rr & 7)) << 5) + ((cc & 1) << 4);
  const unsigned voffW2 = rr * 4096 + swz, voffWo = rr * 2048 + swz, voffL = lane * 16;
  const char* wosrc = (const char*)P.wo + dt * 256;
  const char* osrc = P.ws + WS_OT + (size_t)(stl * 8 + 2 * w4) * 1024;
  const char* wptr = (const char*)P.w2 + stl * 256 + (size_t)(J0 + g) * 32 * 4096;
  const char* lptr = P.ws + WS_UT + (size_t)(J0 + g) * 65536 + (size_t)(dt * 8 + 2 * w4) * 1024;
  if (wid == 0) { poll_n(P, FL_U + dt * 32, 16, nonce); }
  BAR();
  STAMP(10);
#pragma unroll
  for (int r = 0; r < 4; ++r) {
    dma16<0, true>(voffL, lptr + (size_t)r * (2 * 65536), (g + 2 * r) * SLOT + 8192 + 2 * w4 * 1024);
    dma16<1024, true>(voffL, lptr + (size_t)r * (2 * 65536), (g + 2 * r) * SLOT + 8192 + 2 * w4 * 1024);
  }
  wptr += 4 * 64 * 4096; lptr += 4 * 2 * 65536;
  unsigned sbi = g * SLOT;
  auto issue1 = [&](auto WO, int i) {
    if constexpr (!WO.value) {
      if (i == 0) dma16<0, false>(voffW2, wptr, sbi + w4 * 1024);
      else if (i == 1) dma16<0, false>(voffW2, wptr + 16 * 4096, sbi + (w4 + 4) * 1024);
      else if (i == 2) dma16<0, true>(voffL, lptr, sbi + 8192 + 2 * w4 * 1024);
      else { dma16<1024, true>(voffL, lptr, sbi + 8192 + 2 * w4 * 1024); wptr += 2 * 32 * 4096; lptr += 2 * 65536; sbi = (sbi + 2 * SLOT) & (NS * SLOT - 1); }
    } else {
      if (i == 0) dma16<0, false>(voffWo, wptr, sbi + w4 * 1024);
      else if (i == 1) dma16<0, false>(voffWo, wptr + 16 * 2048, sbi + (w4 + 4) * 1024);
      else if (i == 2) dma16<0, true>(voffL, lptr, sbi + 8192 + 2 * w4 * 1024);
      else { dma16<1024, true>(voffL, lptr, sbi + 8192 + 2 * w4 * 1024); wptr += 2 * 32 * 2048; lptr += 2 * 131072; sbi = (sbi + 2 * SLOT) & (NS * SLOT - 1); }
    }
  };
  const int kg = lane >> 4, q = (lane & 15) >> 2, p = lane & 3, r7 = 4 * (kg & 1) + q;
  const unsigned base0 = (4 * kg + q) * 256 + (r7 << 5) + 8 * p;
  unsigned offTs[4], offTd[4];
#pragma unroll
  for (int i = 0; i < 4; ++i) { offTs[i] = base0 ^ ((4 * wn + i) << 5); offTd[i] = base0 ^ ((4 * wm + i) << 5); }
  const unsigned offLd = 8192 + 4 * wm * 1024 + lane * 16, offLs = 8192 + 4 * wn * 1024 + lane * 16;
  float og[4];
#pragma unroll
  for (int i = 0; i < 4; ++i) og[i] = 1.0f - ld_gate(P, 128 * stl + 64 * wn + 16 * i + (lane & 15));
  const int srow = 128 * stl + 64 * wn + 16 * (2 * g + c) + (lane & 15);
  const float b2v = bf2f(P.b2[srow]);
  asm volatile("" : "+v"(og[0]), "+v"(og[1]), "+v"(og[2]), "+v"(og[3]));

  f32x4 acc[4][4];
#pragma unroll
  for (int i = 0; i < 4; ++i)
#pragma unroll
    for (int k = 0; k < 4; ++k) acc[i][k] = (f32x4){0.f, 0.f, 0.f, 0.f};

  WAIT_V(0); BAR();
  STAMP(11);
  unsigned sbr = g * SLOT;
  bf16x8 sA[4], dA[4], sB[4], dB[4];
  auto rd1 = [&](auto RK, int i, LAS char* sl, bf16x8 (&sf)[4], bf16x8 (&df)[4]) {
    if constexpr (RK.value == 0) { if (i < 4) df[i] = rd8(sl + offLd + i * 1024); else sf[i - 4] = cat8(trd(sl + offTs[i - 4]), trd(sl + offTs[i - 4] + 4096)); }
    else { if (i < 4) sf[i] = rd8(sl + offLs + i * 1024); else df[i - 4] = cat8(trd(sl + offTd[i - 4]), trd(sl + offTd[i - 4] + 4096)); }
  };
  {
    LAS char* sl = lds + sbr;
#pragma unroll
    for (int i = 0; i < 8; ++i) rd1(IC<0>{}, i, sl, sA, dA);
    sbr = (sbr + 2 * SLOT) & (NS * SLOT - 1);
  }
  auto step = [&](auto RK, auto DK, auto WAITN, bf16x8 (&sc)[4], bf16x8 (&dc)[4], bf16x8 (&sx)[4], bf16x8 (&dx)[4]) {
    if constexpr (WAITN.value >= 0) wait_v<WAITN.value>();
    LBAR();
    LAS char* sl = lds + sbr;
#pragma unroll
    for (int m = 0; m < 16; ++m) {
      mma16(acc[m >> 2][m & 3], dc[m >> 2], sc[m & 3]);
      SB();
      if constexpr (RK.value < 2) { if (m < 8) rd1(RK, m, sl, sx, dx); }
      if constexpr (DK.value < 2) { if (m >= 9 && m < 16 && (m & 1) == 1) issue1(DK, (m - 9) >> 1); }
      SB();
    }
    sbr = (sbr + 2 * SLOT) & (NS * SLOT - 1);
  };
  auto scale = [&]() {
    MFMA_DONE();
#pragma unroll
    for (int i = 0; i < 4; ++i)
#pragma unroll
      for (int k = 0; k < 4; ++k) acc[i][k] *= og[k];
  };
  using I0 = IC<0>; using I1 = IC<1>; using I2 = IC<2>;
  {
    const int n1 = c ? 8 : 16;
    for (int it = 0; it < n1; ++it) { step(I0{}, I0{}, IC<8>{}, sA, dA, sB, dB); step(I0{}, I0{}, IC<8>{}, sB, dB, sA, dA); }
  }
  if (c == 0) {
    for (int it = opaque(1); it > 0; --it) { step(I0{}, I2{}, IC<8>{}, sA, dA, sB, dB); step(I0{}, I2{}, IC<4>{}, sB, dB, sA, dA); }
    for (int it = opaque(1); it > 0; --it) { step(I0{}, I2{}, IC<0>{}, sA, dA, sB, dB); step(I2{}, I2{}, IC<-1>{}, sB, dB, sA, dA); }
    scale();
  } else {
    if (wid == 0) poll_n(P, FL_O + (stl >> 1) * 32, 16, nonce);
    BAR();
    wptr = wosrc + (size_t)g * (32 * 2048); lptr = osrc + (size_t)g * 131072;
    for (int it = opaque(1); it > 0; --it) { step(I0{}, I1{}, IC<8>{}, sA, dA, sB, dB); step(I0{}, I1{}, IC<8>{}, sB, dB, sA, dA); }
    for (int it = opaque(1); it > 0; --it) { step(I0{}, I1{}, IC<8>{}, sA, dA, sB, dB); step(I1{}, I1{}, IC<8>{}, sB, dB, sA, dA); }
    scale();
    for (int it = opaque(6); it > 0; --it) { step(I1{}, I1{}, IC<8>{}, sA, dA, sB, dB); step(I1{}, I1{}, IC<8>{}, sB, dB, sA, dA); }
    for (int it = opaque(1); it > 0; --it) { step(I1{}, I2{}, IC<8>{}, sA, dA, sB, dB); step(I1{}, I2{}, IC<4>{}, sB, dB, sA, dA); }
    for (int it = opaque(1); it > 0; --it) { step(I1{}, I2{}, IC<0>{}, sA, dA, sB, dB); step(I2{}, I2{}, IC<-1>{}, sB, dB, sA, dA); }
  }
  LBAR();
  STAMP(12);
  const int pair = stl * 8 + dt;
  const __amdgpu_buffer_rsrc_t xs = rsrc(P.ws + WS_XCH + (size_t)(pair * 2 + c) * 32768, 32768), xr = rsrc(P.ws + WS_XCH + (size_t)(pair * 2 + (1 - c)) * 32768, 32768);
  f32x4 keep[4];
  auto red = [&](auto G, auto C) {
    constexpr int gg = G.value, cx = C.value;
#pragma unroll
    for (int i = 0; i < 4; ++i)
#pragma unroll
      for (int e = 0; e < 2; ++e) *(LAS f32x4*)(lds + (wid * 8 + i * 2 + e) * 1024 + lane * 16) = acc[i][2 * (1 - gg) + e];
    LBAR();
#pragma unroll
    for (int i = 0; i < 4; ++i) {
      const f32x4 snd = acc[i][2 * gg + (1 - cx)] + *(LAS f32x4*)(lds + ((wid ^ 4) * 8 + i * 2 + (1 - cx)) * 1024 + lane * 16);
      st16(xs, (wid * 4 + i) * 1024 + lane * 16, __builtin_bit_cast(u32x4, snd));
    }
#pragma unroll
    for (int i = 0; i < 4; ++i) keep[i] = acc[i][2 * gg + cx] + *(LAS f32x4*)(lds + ((wid ^ 4) * 8 + i * 2 + cx) * 1024 + lane * 16);
  };
  if (g == 0) { if (c == 0) red(I0{}, I0{}); else red(I0{}, I1{}); }
  else { if (c == 0) red(I1{}, I0{}); else red(I1{}, I1{}); }
  STAMP(13);
  publish(P, FL_X + pair * 32 + c, nonce);
  if (wid == 0) poll_n(P, FL_X + pair * 32 + (1 - c), 1, nonce);
  BAR();
  STAMP(14);
  {
    const float ogv = 1.0f - ld_gate(P, srow);
    const float bias = ogv * b2v;
    u16* orow = P.out + (size_t)srow * D + 128 * dt + 64 * wm + 4 * kg;
    f32x4 o[4];
#pragma unroll
    for (int i = 0; i < 4; ++i) o[i] = ld16(xr, (wid * 4 + i) * 1024 + lane * 16);
#pragma unroll
    for (int i = 0; i < 4; ++i) {
      const f32x4 v = keep[i] + o[i] + bias;
      *(u32x2*)(orow + 16 * i) = (u32x2){pk2(v[0], v[1]), pk2(v[2], v[3])};
    }
  }
  STAMP(15);
}

__global__ __launch_bounds__(512, 2) void k_fused(Params P) {
  extern __shared__ __attribute__((aligned(16))) char smem[];
  LAS char* lds = (LAS char*)smem;
  const int b = blockIdx.x, xcd = b & 7, loc = b >> 3, i = loc & 15;
  const unsigned nonce = (unsigned)__builtin_amdgcn_readfirstlane(__hip_atomic_load(flagp(P, FL_G), RLX_AGENT)) + 1u;
  FcId fid; fid.stl = 4 * (xcd >> 1) + (i & 3); fid.dt = 4 * (xcd & 1) + (i >> 2); fid.c = loc >> 4;
  if (loc < 16) mlp1_role(P, lds, xcd * 16 + i, 4 * (xcd & 1) + (i & 3), 4 * (xcd >> 1) + (i >> 2), fid, nonce);
  else qkv_role(P, lds, 4 * (xcd & 1) + (i & 3), 4 * (xcd >> 1) + (i >> 2), fid, nonce);
  fc_role(P, lds, fid, nonce);
  if (b == 255 && threadIdx.x == 0) __hip_atomic_store(flagp(P, FL_G), nonce, RLX_AGENT);
}

extern "C" void kernel_launch(void* const* d_in, const int* in_sizes, int n_in, void* d_out, int out_size, void* d_ws, size_t ws_size, hipStream_t stream) {
  Params P;
  P.x = (const u16*)d_in[0]; P.wqkv = (const u16*)d_in[1]; P.wphi = (const u16*)d_in[2]; P.wo = (const u16*)d_in[3];
  P.w1 = (const u16*)d_in[5]; P.b1 = (const u16*)d_in[6]; P.w2 = (const u16*)d_in[7]; P.b2 = (const u16*)d_in[8]; P.gw = (const u16*)d_in[9];
  P.ws = (char*)d_ws; P.out = (u16*)d_out;
  (void)hipFuncSetAttribute((const void*)k_fused, hipFuncAttributeMaxDynamicSharedMemorySize, LDS_BYTES);
  k_fused<<<256, 512, LDS_BYTES, stream>>>(P);
}
```

```cpp
#include <hip/hip_runtime.h>
#include <hip/hip_bf16.h>

typedef unsigned short u16;
typedef short bf16x8 __attribute__((ext_vector_type(8)));
typedef short s16x4 __attribute__((ext_vector_type(4)));
typedef float f32x2 __attribute__((ext_vector_type(2)));
typedef float f32x4 __attribute__((ext_vector_type(4)));
typedef float f32x16 __attribute__((ext_vector_type(16)));
typedef unsigned u32x4 __attribute__((ext_vector_type(4)));
typedef unsigned u32x2 __attribute__((ext_vector_type(2)));
typedef __bf16 bf16x2_t __attribute__((ext_vector_type(2)));
#define LAS __attribute__((address_space(3)))
#define GAS __attribute__((address_space(1)))
#define DI __device__ __forceinline__

constexpr int S = 2048, D = 1024, H = 32, F = 3584;

constexpr size_t WS_FLAGS = 0;
constexpr size_t WS_GATE  = 262144;
constexpr size_t WS_HT    = 1u << 20;
constexpr size_t WS_UT    = WS_HT + (4u << 20);
constexpr size_t WS_OT    = WS_UT + (8u << 20);
constexpr size_t WS_KVP   = WS_OT + (4u << 20);
constexpr size_t WS_ZP    = WS_KVP + (2u << 20);
constexpr size_t WS_XCH   = WS_ZP + (1u << 20);
constexpr size_t WS_DBG   = 64u << 20;
constexpr int FL_G = 0, FL_H = 8, FL_KV = FL_H + 128, FL_O = FL_KV + 128, FL_U = FL_O + 128, FL_X = FL_U + 128;
constexpr unsigned SPIN_MAX = 1u << 18;
constexpr int LDS_BYTES = 147456 + 1024;

struct Params {
  const u16 *x, *wqkv, *wphi, *wo, *w1, *b1, *w2, *b2, *gw;
  char* ws; u16* out;
};
#define STAMP(n) do {} while (0)

DI float bf2f(u16 b) { return __uint_as_float(((unsigned)b) << 16); }
DI float bflo(unsigned w) { return __uint_as_float(w << 16); }
DI float bfhi(unsigned w) { return __uint_as_float(w & 0xffff0000u); }
DI unsigned pk2(float a, float b) { bf16x2_t v = {(__bf16)a, (__bf16)b}; return __builtin_bit_cast(unsigned, v); }
DI bf16x8 mk8(unsigned a, unsigned b, unsigned c, unsigned d) { u32x4 v = {a, b, c, d}; return __builtin_bit_cast(bf16x8, v); }
DI bf16x8 cat8(s16x4 lo, s16x4 hi) { return __builtin_shufflevector(lo, hi, 0, 1, 2, 3, 4, 5, 6, 7); }

template <int IMM, bool SC1> DI void dma16(unsigned voff, const void* sbase, unsigned ldsaddr) {
  if (SC1) asm volatile("s_mov_b32 m0, %2\n\ts_nop 0\n\tglobal_load_lds_dwordx4 %0, %1 offset:%3 sc1" ::"v"(voff), "s"(sbase), "s"(ldsaddr), "i"(IMM) : "memory");
  else asm volatile("s_mov_b32 m0, %2\n\ts_nop 0\n\tglobal_load_lds_dwordx4 %0, %1 offset:%3" ::"v"(voff), "s"(sbase), "s"(ldsaddr), "i"(IMM) : "memory");
}
DI void pf8k(unsigned voff, const void* sbase) {
  asm volatile("s_mov_b32 m0, %2\n\ts_nop 0\n\tglobal_load_lds_dword %0, %1" ::"v"(voff), "s"(sbase), "s"(147456u + 512u) : "memory");
}
template <int N> DI void wait_v() { asm volatile("s_waitcnt vmcnt(%0)" ::"n"(N) : "memory"); }
template <int V> struct IC { static constexpr int value = V; };
#define SB() __builtin_amdgcn_sched_barrier(0)
#define WAIT_V(n) asm volatile("s_waitcnt vmcnt(" #n ")" ::: "memory")
#define WAIT_L0() asm volatile("s_waitcnt lgkmcnt(0)" ::: "memory")
#define BAR() do { asm volatile("s_barrier" ::: "memory"); SB(); } while (0)
#define LBAR() do { asm volatile("s_waitcnt lgkmcnt(0)\n\ts_barrier" ::: "memory"); SB(); } while (0)
DI s16x4 trd(LAS char* p) { return __builtin_amdgcn_ds_read_tr16_b64_v4i16((LAS s16x4*)p); }
DI bf16x8 rd8(LAS char* p) { return *(LAS bf16x8*)p; }
#define MFMA16(a, b, c) __builtin_amdgcn_mfma_f32_16x16x32_bf16((a), (b), (c), 0, 0, 0)
#define MFMA32(a, b, c) __builtin_amdgcn_mfma_f32_32x32x16_bf16((a), (b), (c), 0, 0, 0)

DI void mma16(f32x4& c, const bf16x8& a, const bf16x8& b) { c = MFMA16(a, b, c); }
DI void mma32(f32x16& c, const bf16x8& a, const bf16x8& b) { c = MFMA32(a, b, c); }
#define MFMA_DONE() SB()
DI int opaque(int n) { asm volatile("" : "+s"(n)); return n; }
template <int CTRL> DI float dpp_mov(float v) { return __builtin_bit_cast(float, __builtin_amdgcn_update_dpp(0, __builtin_bit_cast(int, v), CTRL, 0xF, 0xF, true)); }
DI f32x2 pair32(float v) { const unsigned u = __float_as_uint(v); auto r = __builtin_amdgcn_permlane32_swap(u, u, false, false); return (f32x2){__uint_as_float(r[0]), __uint_as_float(r[1])}; }
DI f32x2 pair16(float v) { const unsigned u = __float_as_uint(v); auto r = __builtin_amdgcn_permlane16_swap(u, u, false, false); return (f32x2){__uint_as_float(r[0]), __uint_as_float(r[1])}; }
DI float wave_sum(float v) {
  v += dpp_mov<0xB1>(v); v += dpp_mov<0x4E>(v); v += dpp_mov<0x141>(v); v += dpp_mov<0x140>(v);
  f32x2 a = pair16(v); v = a.x + a.y;
  f32x2 b = pair32(v); return b.x + b.y;
}

#define RLX_AGENT __ATOMIC_RELAXED, __HIP_MEMORY_SCOPE_AGENT
DI GAS unsigned* flagp(const Params& P, int w) { return (GAS unsigned*)(P.ws + WS_FLAGS) + w * 32; }
DI void publish(const Params& P, int w, unsigned nonce) {
  WAIT_V(0); BAR();
  if (threadIdx.x == 0) __hip_atomic_store(flagp(P, w), nonce, RLX_AGENT);
}
DI void poll_n(const Params& P, int w0, int n, unsigned nonce) {
  const int lane = threadIdx.x & 63;
  GAS unsigned* p0 = flagp(P, w0 + (lane < n ? lane : 0));
  GAS unsigned* p1 = flagp(P, w0 + (lane + 64 < n ? lane + 64 : 0));
  for (unsigned spins = 0; spins < SPIN_MAX; ++spins) {
    const unsigned a = __hip_atomic_load(p0, RLX_AGENT), b = __hip_atomic_load(p1, RLX_AGENT);
    if (__all(a == nonce && b == nonce)) break;
    __builtin_amdgcn_s_sleep(1);
  }
}
DI float ld_gate(const Params& P, int tok) { return __uint_as_float(__hip_atomic_load((GAS unsigned*)(P.ws + WS_GATE) + tok, RLX_AGENT)); }
DI __amdgpu_buffer_rsrc_t rsrc(const void* p, unsigned bytes) { return __builtin_amdgcn_make_buffer_rsrc((void*)p, 0, bytes, 0x00020000); }
DI void st16(__amdgpu_buffer_rsrc_t r, unsigned off, u32x4 v) { __builtin_amdgcn_raw_buffer_store_b128(v, r, off, 0, 16); }
DI f32x4 ld16(__amdgpu_buffer_rsrc_t r, unsigned off) { return __builtin_bit_cast(f32x4, __builtin_amdgcn_raw_buffer_load_b128(r, off, 0, 16)); }

template <class CB> DI void ln_unit(const Params& P, LAS char* lds, int m, CB&& after_loads) {
  const int tid = threadIdx.x, lane = tid & 63, w = tid >> 6;
  const int kb = m >> 1, hf = m & 1;
  auto xrow = [&](int i) { return lds + (i >> 2) * 24576 + 16384 + (i & 3) * 2048; };
  LAS f32x2* st = (LAS f32x2*)(lds + 147456);
  u32x4 v[2][2];
#pragma unroll
  for (int e = 0; e < 2; ++e) {
    const int i = 2 * w + e, tok = 32 * kb + (i < 8 ? 8 * hf + i : 16 + 8 * hf + (i - 8));
    v[e][0] = *(const u32x4*)(P.x + (size_t)tok * D + lane * 8);
    v[e][1] = *(const u32x4*)(P.x + (size_t)tok * D + 512 + lane * 8);
  }
  const u32x4 g0 = *(const u32x4*)(P.gw + lane * 8), g1 = *(const u32x4*)(P.gw + 512 + lane * 8);
#pragma unroll
  for (int e = 0; e < 2; ++e) {
    const int i = 2 * w + e, tok = 32 * kb + (i < 8 ? 8 * hf + i : 16 + 8 * hf + (i - 8));
    float s = 0.f, q = 0.f, gd = 0.f;
#pragma unroll
    for (int c = 0; c < 2; ++c)
#pragma unroll
      for (int j = 0; j < 4; ++j) {
        const unsigned xw = v[e][c][j], gw = c ? g1[j] : g0[j];
        const float a = bflo(xw), b = bfhi(xw);
        s += a + b; q += a * a + b * b; gd += a * bflo(gw) + b * bfhi(gw);
      }
    s = wave_sum(s); q = wave_sum(q); gd = wave_sum(gd);
    const float mean = s * (1.0f / D);
    const float var = fmaxf(q * (1.0f / D) - mean * mean, 0.f);
    const float rstd = 1.0f / sqrtf(var + 1e-5f);
    *(LAS u32x4*)(xrow(i) + lane * 16) = v[e][0];
    *(LAS u32x4*)(xrow(i) + 1024 + lane * 16) = v[e][1];
    if (lane == 0) {
      st[i] = (f32x2){mean, rstd};
      __hip_atomic_store((GAS unsigned*)(P.ws + WS_GATE) + tok, __float_as_uint(1.0f / (1.0f + __expf(-gd))), RLX_AGENT);
    }
    if (e == 0) { asm volatile("" : "+v"(v[1][0]), "+v"(v[1][1])); after_loads(); }
  }
  LBAR();
  float h0[16], h1[16];
#pragma unroll
  for (int i = 0; i < 16; ++i) {
    const unsigned xw = *(LAS unsigned*)(xrow(i) + tid * 4); const f32x2 s = st[i];
    h0[i] = (bflo(xw) - s.x) * s.y; h1[i] = (bfhi(xw) - s.x) * s.y;
  }
  const int d = 2 * tid;
  const __amdgpu_buffer_rsrc_t rs = rsrc(P.ws + WS_HT, 4u << 20);
  const unsigned dst = ((kb * 64 + (d >> 4)) * 64 + (d & 15)) * 16;
#pragma unroll
  for (int e = 0; e < 2; ++e) {
    const int kg = 2 * hf + e, a = 4 * e, b = 8 + 4 * e;
    st16(rs, dst + kg * 256, (u32x4){pk2(h0[a], h0[a + 1]), pk2(h0[a + 2], h0[a + 3]), pk2(h0[b], h0[b + 1]), pk2(h0[b + 2], h0[b + 3])});
    st16(rs, dst + kg * 256 + 16, (u32x4){pk2(h1[a], h1[a + 1]), pk2(h1[a + 2], h1[a + 3]), pk2(h1[b], h1[b + 1]), pk2(h1[b + 2], h1[b + 3])});
  }
}

namespace fc { constexpr int SLOT = 16384, NS = 8; }
struct FcId { int stl, dt, c; };
DI void fc_prologue_w(const Params& P, const FcId id) {
  using namespace fc;
  const int lane = threadIdx.x & 63, wid = __builtin_amdgcn_readfirstlane(threadIdx.x >> 6), g = wid >> 2, w4 = wid & 3;
  const int rr = 4 * w4 + (lane >> 4), cc = lane & 15;
  const unsigned voffW2 = rr * 4096 + (((cc >> 1) ^ (rr & 7)) << 5) + ((cc & 1) << 4);
  const char* src = (const char*)P.w2 + id.stl * 256 + (size_t)((id.c ? 72 : 0) + g) * 32 * 4096;
#pragma unroll
  for (int r = 0; r < 4; ++r) {
    const unsigned sb = (g + 2 * r) * SLOT;
    dma16<0, false>(voffW2, src + (size_t)r * (64 * 4096), sb + w4 * 1024);
    dma16<0, false>(voffW2, src + (size_t)r * (64 * 4096) + 16 * 4096, sb + (w4 + 4) * 1024);
  }
}

namespace m1 { constexpr int SLOT = 24576, NS = 6; }
DI f32x2 gelu2(f32x2 x) {
  constexpr float C0 = -2.885390081777927f * 0.7978845608028654f, C1 = C0 * 0.044715f;
  const f32x2 t = x * x * C1 + C0;
  const f32x2 a = x * t;
  f32x2 e = {__builtin_amdgcn_exp2f(a.x), __builtin_amdgcn_exp2f(a.y)};
  e = e + 1.0f;
  const f32x2 r = {__builtin_amdgcn_rcpf(e.x), __builtin_amdgcn_rcpf(e.y)};
  return x * r;
}
DI void mlp1_role(const Params& P, LAS char* lds, int m, int dt, int ft, const FcId fid, unsigned nonce) {
  using namespace m1;
  const int tid = threadIdx.x, lane = tid & 63;
  const int wid = __builtin_amdgcn_readfirstlane(tid >> 6);
  const int g = wid >> 2, w4 = wid & 3, wm = w4 >> 1, wn = w4 & 1, odd = ft & 1;
  const char* wptr = (const char*)P.w1 + (224 * ft - 32 * odd) * 2 + (size_t)g * (32 * 7168);
  const char* hptr = P.ws + WS_HT + (size_t)(dt * 8 + 2 * w4) * 1024 + (size_t)g * 65536;
  const int r0 = 2 * w4 + (lane >> 5), c = lane & 31;
  const unsigned voffW = r0 * 7168 + (((c >> 1) ^ r0) << 5) + ((c & 1) << 4);
  const unsigned voffH = lane * 16;
  STAMP(0);
  ln_unit(P, lds, m, [&]() {
#pragma unroll
    for (int r = 0; r < 3; ++r)
#pragma unroll
      for (int i = 0; i < 4; ++i) dma16<0, false>(voffW, wptr + (size_t)r * (64 * 7168) + i * (8 * 7168), (g + 2 * r) * SLOT + (w4 + 4 * i) * 1024);
  });
  STAMP(1);
  publish(P, FL_H + m, nonce);
  if (wid == 0) poll_n(P, FL_H, 128, nonce);
  BAR();
  STAMP(2);
#pragma unroll
  for (int r = 0; r < 3; ++r) {
    dma16<0, true>(voffH, hptr + (size_t)r * (2 * 65536), (g + 2 * r) * SLOT + 16384 + 2 * w4 * 1024);
    dma16<1024, true>(voffH, hptr + (size_t)r * (2 * 65536), (g + 2 * r) * SLOT + 16384 + 2 * w4 * 1024);
  }
  wptr += 3 * 64 * 7168; hptr += 3 * 2 * 65536;
  unsigned sbi = g * SLOT;
  auto issue1 = [&](int i) {
    if (i < 4) dma16<0, false>(voffW, wptr + i * (8 * 7168), sbi + (w4 + 4 * i) * 1024);
    else if (i == 4) dma16<0, true>(voffH, hptr, sbi + 16384 + 2 * w4 * 1024);
    else {
      dma16<1024, true>(voffH, hptr, sbi + 16384 + 2 * w4 * 1024);
      wptr += 2 * 32 * 7168; hptr += 2 * 65536;
      sbi += 2 * SLOT; sbi = sbi >= NS * SLOT ? sbi - NS * SLOT : sbi;
    }
  };
  const int kg = lane >> 4, q = (lane & 15) >> 2, p = lane & 3, r7 = 4 * (kg & 1) + q;
  const unsigned base0 = (4 * kg + q) * 512 + (r7 << 5) + 8 * p;
  const int u0 = 7 * wn + 2 * odd;
  unsigned offW[7];
#pragma unroll
  for (int i = 0; i < 7; ++i) offW[i] = base0 ^ ((u0 + i) << 5);
  const unsigned offH = 16384 + 4 * wm * 1024 + lane * 16;

  f32x4 acc[7][4];
#pragma unroll
  for (int i = 0; i < 7; ++i)
#pragma unroll
    for (int d = 0; d < 4; ++d) acc[i][d] = (f32x4){0.f, 0.f, 0.f, 0.f};

  WAIT_V(0); BAR();
  STAMP(3);
  unsigned sbr = g * SLOT;
  bf16x8 wA[7], hA[4], wB[7], hB[4];
  auto rd1 = [&](int i, LAS char* sl, bf16x8 (&wf)[7], bf16x8 (&hf)[4]) {
    if (i < 4) hf[i] = rd8(sl + offH + i * 1024);
    else wf[i - 4] = cat8(trd(sl + offW[i - 4]), trd(sl + offW[i - 4] + 8192));
  };
  {
    LAS char* sl = lds + sbr;
#pragma unroll
    for (int i = 0; i < 11; ++i) rd1(i, sl, wA, hA);
    sbr += 2 * SLOT; sbr = sbr >= NS * SLOT ? sbr - NS * SLOT : sbr;
  }
  auto step = [&](auto READ, auto ISSUE, auto WAITN, bf16x8 (&wc)[7], bf16x8 (&hc)[4], bf16x8 (&wx)[7], bf16x8 (&hx)[4]) {
    if constexpr (WAITN.value >= 0) wait_v<WAITN.value>();
    LBAR();
    LAS char* sl = lds + sbr;
#pragma unroll
    for (int mm = 0; mm < 28; ++mm) {
      mma16(acc[mm >> 2][mm & 3], wc[mm >> 2], hc[mm & 3]);
      SB();
      if constexpr (READ.value) { if (mm < 11) rd1(mm, sl, wx, hx); }
      if constexpr (ISSUE.value) { if (mm >= 12 && mm < 24 && (mm & 1) == 0) issue1((mm - 12) >> 1); }
      SB();
    }
    sbr += 2 * SLOT; sbr = sbr >= NS * SLOT ? sbr - NS * SLOT : sbr;
  };
  for (int it = 0; it < 14; ++it) { step(IC<1>{}, IC<1>{}, IC<6>{}, wA, hA, wB, hB); step(IC<1>{}, IC<1>{}, IC<6>{}, wB, hB, wA, hA); }
  for (int it = opaque(1); it > 0; --it) { step(IC<1>{}, IC<1>{}, IC<6>{}, wA, hA, wB, hB); step(IC<1>{}, IC<0>{}, IC<6>{}, wB, hB, wA, hA); }
  for (int it = opaque(1); it > 0; --it) { step(IC<1>{}, IC<0>{}, IC<0>{}, wA, hA, wB, hB); step(IC<0>{}, IC<0>{}, IC<-1>{}, wB, hB, wA, hA); }
  MFMA_DONE();
  LBAR();
  STAMP(4);
  auto xaddr = [&](int w, int e) { return lds + (w * 15 + e) * 1024 + lane * 16; };
  const int wP = wid ^ 4, wQ0 = wid ^ 1, wQ1 = wid ^ 5;
  const __amdgpu_buffer_rsrc_t urs = rsrc(P.ws + WS_UT, 8u << 20);
  const unsigned utile = (7 * ft) * 65536 + (dt * 8 + 4 * wm + 2 * g) * 1024 + lane * 16;
  const u16* b1p = P.b1 + 224 * ft + 4 * kg;
  auto epi = [&](auto G, auto WN) {
    constexpr int gg = G.value, wnn = WN.value, IM = wnn ? 0 : 6, IMQ = wnn ? 6 : 0;
    u32x2 bias[7];
#pragma unroll
    for (int i = 0; i < 7; ++i) bias[i] = *(const u32x2*)(b1p + 16 * (7 * wnn + i));
    const u32x2 biasq = *(const u32x2*)(b1p + 16 * (wnn ? 6 : 7));
#pragma unroll
    for (int dd = 0; dd < 2; ++dd)
#pragma unroll
      for (int i = 0; i < 7; ++i) *(LAS f32x4*)xaddr(wid, dd * 7 + i) = acc[i][2 * (1 - gg) + dd];
    *(LAS f32x4*)xaddr(wid, 14) = acc[IM][2 * gg + (1 - wnn)];
    LBAR();
    STAMP(16);
    f32x4 t[2][7], tq;
#pragma unroll
    for (int dd = 0; dd < 2; ++dd)
#pragma unroll
      for (int i = 0; i < 7; ++i)
        if (i != IM || dd == wnn) t[dd][i] = acc[i][2 * gg + dd] + *(LAS f32x4*)xaddr(wP, dd * 7 + i);
    tq = *(LAS f32x4*)xaddr(wQ0, 14) + *(LAS f32x4*)xaddr(wQ1, wnn * 7 + IMQ);
    LBAR();
    asm volatile("" : "+v"(bias[0]), "+v"(bias[1]), "+v"(bias[2]), "+v"(bias[3]), "+v"(bias[4]), "+v"(bias[5]), "+v"(bias[6]));
    u32x2 bq = biasq; asm volatile("" : "+v"(bq));
    STAMP(17);
    fc_prologue_w(P, fid);
    auto fin = [&](f32x4 v, u32x2 bw) -> u32x2 {
      const f32x2 a = gelu2((f32x2){v[0] + bflo(bw[0]), v[1] + bfhi(bw[0])}), b = gelu2((f32x2){v[2] + bflo(bw[1]), v[3] + bfhi(bw[1])});
      return (u32x2){pk2(a.x, a.y), pk2(b.x, b.y)};
    };
#pragma unroll
    for (int dd = 0; dd < 2; ++dd)
#pragma unroll
      for (int pr = 0; pr < 3; ++pr) {
        const int i0 = 2 * pr + wnn, b = 7 * wnn + i0;
        const u32x2 lo = fin(t[dd][i0], bias[i0]), hi = fin(t[dd][i0 + 1], bias[i0 + 1]);
        st16(urs, utile + (b >> 1) * 65536 + dd * 1024, (u32x4){lo[0], lo[1], hi[0], hi[1]});
      }
    {
      const u32x2 mine = fin(t[wnn][IM], bias[IM]), oth = fin(tq, bq);
      st16(urs, utile + 3 * 65536 + wnn * 1024, wnn ? (u32x4){oth[0], oth[1], mine[0], mine[1]} : (u32x4){mine[0], mine[1], oth[0], oth[1]});
    }
  };
  if (g == 0) { if (wn == 0) epi(IC<0>{}, IC<0>{}); else epi(IC<0>{}, IC<1>{}); }
  else { if (wn == 0) epi(IC<1>{}, IC<0>{}); else epi(IC<1>{}, IC<1>{}); }
  STAMP(5);
  publish(P, FL_U + dt * 16 + ft, nonce);
  STAMP(6);
}

namespace qk { constexpr int SLOT = 57344, WOFF = 32768, NT = 16; }
DI void softmax_pm(const f32x16& pq, u32x4 (&out)[4]) {
  float mx = fmaxf(pq[0], pq[1]), mn = fminf(pq[0], pq[1]);
#pragma unroll
  for (int i = 2; i < 16; i += 2) { mx = fmaxf(fmaxf(mx, pq[i]), pq[i + 1]); mn = fminf(fminf(mn, pq[i]), pq[i + 1]); }
  { const f32x2 a = pair32(mx), b = pair32(mn); mx = fmaxf(a.x, a.y); mn = fminf(b.x, b.y); }
  constexpr float L2E = 1.4426950408889634f;
  const float mxs = -mx * L2E, mns = mn * L2E;
  f32x2 ep[8], en[8], sp = {0.f, 0.f}, sn = {0.f, 0.f};
#pragma unroll
  for (int i = 0; i < 8; ++i) {
    const f32x2 pv = {pq[2 * i], pq[2 * i + 1]};
    const f32x2 a = pv * L2E + mxs, b = mns - pv * L2E;
    ep[i] = (f32x2){__builtin_amdgcn_exp2f(a.x), __builtin_amdgcn_exp2f(a.y)};
    en[i] = (f32x2){__builtin_amdgcn_exp2f(b.x), __builtin_amdgcn_exp2f(b.y)};
    sp += ep[i]; sn += en[i];
  }
  const f32x2 c = pair32(sp.x + sp.y), d = pair32(sn.x + sn.y);
  const float rp = __builtin_amdgcn_rcpf(c.x + c.y), rn = __builtin_amdgcn_rcpf(d.x + d.y);
#pragma unroll
  for (int s = 0; s < 2; ++s)
#pragma unroll
    for (int j = 0; j < 4; ++j) {
      const f32x2 a = ep[4 * s + j] * rp, b = en[4 * s + j] * rn;
      out[s][j] = pk2(a.x, a.y); out[2 + s][j] = pk2(b.x, b.y);
    }
}
DI bf16x8 pack8(const f32x16& x, int s) {
  return mk8(pk2(x[8 * s], x[8 * s + 1]), pk2(x[8 * s + 2], x[8 * s + 3]), pk2(x[8 * s + 4], x[8 * s + 5]), pk2(x[8 * s + 6], x[8 * s + 7]));
}
DI void qkv_role(const Params& P, LAS char* lds, int st, int hp, int jq, const FcId fid, unsigned nonce) {
  using namespace qk;
  const int tid = threadIdx.x, lane = tid & 63;
  const int wid = __builtin_amdgcn_readfirstlane(tid >> 6);
  const int g = wid >> 2, w4 = wid & 3, wm2 = w4 >> 1, wn = w4 & 1;
  const int head = 2 * hp + wn, tokb = 128 * g + 64 * wm2;
  const int h2 = lane >> 5, r = lane & 31;
  unsigned voffW[3];
#pragma unroll
  for (int b = 0; b < 3; ++b) {
    const int li = 8 * b + (lane >> 3), row = li / 3, part = li % 3;
    voffW[b] = row * 6144 + part * 2048 + (((lane & 7) ^ (((row >> 1) & 1) << 2)) << 4);
  }
  const unsigned voffX = (lane >> 3) * 2048 + (((lane & 7) ^ ((4 * (wid & 1) + (lane >> 4)) & 7)) << 4);
  const char* wsrc = (const char*)P.wqkv + hp * 128;
  const char* xsrc = (const char*)P.x + (size_t)(256 * st) * 2048;
  const bool stager = wid != 7;
  const int xe = 7 + 8 * (wid - 3);
  const unsigned voffE = wid < 3 ? (wid == 0 ? voffW[0] : (wid == 1 ? voffW[1] : voffW[2])) : (lane >> 3) * 2048 + (((lane & 7) ^ ((4 + (lane >> 4)) & 7)) << 4);
  const char* ebase = wid < 3 ? wsrc + (size_t)56 * 6144 : xsrc + (size_t)xe * 16384;
  const unsigned estep = wid < 3 ? 64 * 6144 : 128, elds = wid < 3 ? WOFF + (21 + wid) * 1024 : xe * 1024;
  auto issue1 = [&](int t, int i) {
    const unsigned sb = (t & 1) * SLOT;
    if (i < 3) dma16<0, false>(voffW[i], wsrc + (size_t)(64 * t + 8 * wid) * 6144, sb + WOFF + (3 * wid + i) * 1024);
    else if (i < 7) dma16<0, false>(voffX, xsrc + (size_t)(wid + 8 * (i - 3)) * 16384 + t * 128, sb + (wid + 8 * (i - 3)) * 1024);
    else dma16<0, false>(voffE, ebase + (size_t)t * estep, sb + elds);
  };
  const unsigned voffP = lane * 128;
  const size_t pfo = (size_t)jq * 8192;
#define PF_SEL(r) true
  constexpr size_t MB = 1u << 20;
  STAMP(0);
  if (stager) {
#pragma unroll
    for (int i = 0; i < 8; ++i) issue1(0, i);
#pragma unroll
    for (int i = 0; i < 8; ++i) issue1(1, i);
  } else {
    if (PF_SEL(0)) { pf8k(voffP, (const char*)P.wqkv + pfo); pf8k(voffP, (const char*)P.w1 + pfo); }
    if (PF_SEL(1)) { pf8k(voffP, (const char*)P.wqkv + MB + pfo); pf8k(voffP, (const char*)P.w1 + MB + pfo); }
  }
  bf16x8 wphi[2];
#pragma unroll
  for (int s = 0; s < 2; ++s)
#pragma unroll
    for (int j = 0; j < 8; ++j) wphi[s][j] = (short)P.wphi[(head * 32 + 16 * s + 8 * (j >> 2) + 4 * h2 + (j & 3)) * 32 + r];
  asm volatile("" : "+v"(wphi[0]), "+v"(wphi[1]));
  unsigned offX[4];
#pragma unroll
  for (int s = 0; s < 4; ++s) offX[s] = (tokb + r) * 128 + (((2 * s + h2) ^ ((r >> 1) & 7)) << 4);
  const int blk = (lane >> 4) & 1, q = (lane & 15) >> 2, p = lane & 3;
  const unsigned offW = WOFF + (8 * h2 + q) * 384 + 64 * (wn ^ (q >> 1)) + 32 * blk + 8 * p;

  f32x16 aq[2], ak[2], av[2];
#pragma unroll
  for (int t = 0; t < 2; ++t)
#pragma unroll
    for (int i = 0; i < 16; ++i) { aq[t][i] = 0.f; ak[t][i] = 0.f; av[t][i] = 0.f; }

  if (stager) WAIT_V(0);
  BAR();
  STAMP(3);
  bf16x8 xA[2][2], wA[3][2], xB[2][2], wB[3][2];
  auto rd1 = [&](int i, LAS char* sl, int hf, bf16x8 (&xf)[2][2], bf16x8 (&wf)[3][2]) {
    if (i < 4) xf[i & 1][i >> 1] = rd8(sl + offX[2 * hf + (i >> 1)] + (i & 1) * 4096);
    else { const int part = (i - 4) % 3, s2 = (i - 4) / 3, s = 2 * hf + s2; wf[part][s2] = cat8(trd(sl + offW + (16 * s) * 384 + part * 128), trd(sl + offW + (16 * s + 4) * 384 + part * 128)); }
  };
#pragma unroll
  for (int i = 0; i < 10; ++i) rd1(i, lds, 0, xA, wA);
  auto half = [&](auto READ, auto ISSUE, LAS char* sl, int hf, int tn, bf16x8 (&xc)[2][2], bf16x8 (&wc)[3][2], bf16x8 (&xx)[2][2], bf16x8 (&wx)[3][2]) {
#pragma unroll
    for (int m = 0; m < 12; ++m) {
      const int s2 = m / 6, tb = (m % 6) / 3, part = m % 3;
      if (part == 0) mma32(aq[tb], wc[0][s2], xc[tb][s2]);
      else if (part == 1) mma32(ak[tb], wc[1][s2], xc[tb][s2]);
      else mma32(av[tb], xc[tb][s2], wc[2][s2]);
      SB();
      if constexpr (READ.value) { if (m < 10) rd1(m, sl, hf, xx, wx); }
      if constexpr (ISSUE.value == 1) {
        if (m < 8) { if (stager) issue1(tn, m);
          else if (m == 0) { const int t = tn - 2; if (PF_SEL(t)) pf8k(voffP, (t < 12 ? (const char*)P.w1 + (size_t)(2 + t) * MB : (const char*)P.wo + (size_t)(t - 12) * MB) + pfo); }
          else if (m == 4) { const int t = tn - 2; if (PF_SEL(t)) pf8k(voffP, (t < 4 ? (const char*)P.wqkv + (size_t)(2 + t) * MB : (const char*)P.w2 + (size_t)(t - 4) * MB) + pfo); }
        }
      }
      if constexpr (ISSUE.value == 2) { if (!stager && (m == 0 || m == 6) && PF_SEL(m / 6)) pf8k(voffP, (const char*)P.w2 + (size_t)(tn + m / 6) * MB + pfo); }
      SB();
    }
  };
  for (int t = 0; t < NT - 2; ++t) {
    LAS char* s0 = lds + (t & 1) * SLOT; LAS char* s1 = lds + ((t + 1) & 1) * SLOT;
    half(IC<1>{}, IC<0>{}, s0, 1, 0, xA, wA, xB, wB);
    if (stager) WAIT_V(0);
    LBAR();
    half(IC<1>{}, IC<1>{}, s1, 0, t + 2, xB, wB, xA, wA);
  }
  for (int it = opaque(1); it > 0; --it) {
    half(IC<1>{}, IC<2>{}, lds, 1, 10, xA, wA, xB, wB);
    if (stager) WAIT_V(0);
    LBAR();
    half(IC<1>{}, IC<2>{}, lds + SLOT, 0, 12, xB, wB, xA, wA);
  }
  for (int it = opaque(1); it > 0; --it) {
    half(IC<1>{}, IC<0>{}, lds + SLOT, 1, 0, xA, wA, xB, wB);
    half(IC<0>{}, IC<0>{}, lds, 0, 0, xB, wB, xA, wA);
  }
  MFMA_DONE();
  LBAR();
  STAMP(4);
  LAS char* img = lds + wid * 8192;
  f32x16 zero;
#pragma unroll
  for (int i = 0; i < 16; ++i) zero[i] = 0.f;
  const unsigned sw = (r & 1) | (((r >> 2) & 3) << 1);
#pragma unroll
  for (int tb = 0; tb < 2; ++tb) {
    f32x16 pk = MFMA32(wphi[0], pack8(ak[tb], 0), zero); pk = MFMA32(wphi[1], pack8(ak[tb], 1), pk);
    u32x4 ph[4]; softmax_pm(pk, ph);
#pragma unroll
    for (int fb = 0; fb < 2; ++fb)
#pragma unroll
      for (int g4 = 0; g4 < 4; ++g4) {
        const u32x2 w = {ph[2 * fb + (g4 >> 1)][2 * (g4 & 1)], ph[2 * fb + (g4 >> 1)][2 * (g4 & 1) + 1]};
        *(LAS u32x2*)(img + (32 * tb + r) * 128 + 64 * (fb ^ ((r >> 1) & 1)) + 8 * ((2 * g4 + h2) ^ sw)) = w;
      }
  }
  WAIT_L0();
  STAMP(16);
  f32x16 kv[2] = {zero, zero}, zz[2] = {zero, zero};
  const bf16x8 ones = mk8(0x3f803f80u, 0x3f803f80u, 0x3f803f80u, 0x3f803f80u);
#pragma unroll
  for (int tb = 0; tb < 2; ++tb)
#pragma unroll
    for (int s = 0; s < 2; ++s) {
      const bf16x8 vb = pack8(av[tb], s);
#pragma unroll
      for (int fb = 0; fb < 2; ++fb) {
        s16x4 a[2];
#pragma unroll
        for (int t = 0; t < 2; ++t) {
          const int row = 16 * s + 8 * t + 4 * h2 + q;
          const unsigned swr = (row & 1) | (((row >> 2) & 3) << 1);
          a[t] = trd(img + (32 * tb + row) * 128 + 64 * (fb ^ ((row >> 1) & 1)) + 8 * ((4 * blk + p) ^ swr));
        }
        const bf16x8 af = cat8(a[0], a[1]);
        kv[fb] = MFMA32(af, vb, kv[fb]); zz[fb] = MFMA32(af, ones, zz[fb]);
      }
    }
  STAMP(17);
  LAS char* red = lds + 65536;
  constexpr int RS = 8448;
#pragma unroll
  for (int fb = 0; fb < 2; ++fb) {
#pragma unroll
    for (int c = 0; c < 4; ++c) *(LAS f32x4*)(red + wid * RS + (fb * 4 + c) * 1024 + lane * 16) = (f32x4){kv[fb][4 * c], kv[fb][4 * c + 1], kv[fb][4 * c + 2], kv[fb][4 * c + 3]};
    if (r == 0) {
#pragma unroll
      for (int c = 0; c < 4; ++c) *(LAS f32x4*)(red + wid * RS + 8192 + (fb * 2 + h2) * 64 + c * 16) = (f32x4){zz[fb][4 * c], zz[fb][4 * c + 1], zz[fb][4 * c + 2], zz[fb][4 * c + 3]};
    }
  }
  LBAR();
  const int qd = 2 * g + wm2;
  const __amdgpu_buffer_rsrc_t kvrs = rsrc(P.ws + WS_KVP, 2u << 20), zrs = rsrc(P.ws + WS_ZP, 65536);
  {
#pragma unroll
    for (int e = 0; e < 2; ++e) {
      const int ch = 2 * qd + e;
      f32x4 s = (f32x4){0.f, 0.f, 0.f, 0.f};
#pragma unroll
      for (int w = 0; w < 4; ++w) s += *(LAS f32x4*)(red + (2 * w + wn) * RS + ch * 1024 + lane * 16);
      st16(kvrs, (st * 32 + head) * 8192 + ch * 1024 + lane * 16, __builtin_bit_cast(u32x4, s));
    }
    if (qd == 0 && lane < 16) {
      f32x4 s = (f32x4){0.f, 0.f, 0.f, 0.f};
#pragma unroll
      for (int w = 0; w < 4; ++w) s += *(LAS f32x4*)(red + (2 * w + wn) * RS + 8192 + lane * 16);
      st16(zrs, ((st * 32 + head) * 64 + lane * 4) * 4, __builtin_bit_cast(u32x4, s));
    }
  }
  WAIT_L0();
  STAMP(18);
  publish(P, FL_KV + hp * 8 + st, nonce);
  STAMP(5);
  fc_prologue_w(P, fid);
  u32x4 phq[2][4];
#pragma unroll
  for (int tb = 0; tb < 2; ++tb) {
    f32x16 pq = MFMA32(wphi[0], pack8(aq[tb], 0), zero); pq = MFMA32(wphi[1], pack8(aq[tb], 1), pq);
    softmax_pm(pq, phq[tb]);
  }
  STAMP(6);
  if (wid == 0) { poll_n(P, FL_KV + hp * 8, 8, nonce); poll_n(P, FL_H, 128, nonce); }
  BAR();
  STAMP(7);
  LAS char* osc = lds + 131072;
  {
    const int ks = qd, fb = ks >> 1, s = ks & 1;
    const unsigned ko = head * 8192 + (fb * 4 + 2 * s) * 1024 + lane * 16, zo = (head * 64 + (fb * 2 + h2) * 16 + 8 * s) * 4;
    f32x4 a0[8], a1[8], z0[8], z1[8];
#pragma unroll
    for (int t = 0; t < 8; ++t) { a0[t] = ld16(kvrs, ko + t * (32 * 8192)); a1[t] = ld16(kvrs, ko + t * (32 * 8192) + 1024); z0[t] = ld16(zrs, zo + t * (32 * 256)); z1[t] = ld16(zrs, zo + t * (32 * 256) + 16); }
    const f32x4 sa0 = ((a0[0] + a0[1]) + (a0[2] + a0[3])) + ((a0[4] + a0[5]) + (a0[6] + a0[7])), sa1 = ((a1[0] + a1[1]) + (a1[2] + a1[3])) + ((a1[4] + a1[5]) + (a1[6] + a1[7]));
    const f32x4 sz0 = ((z0[0] + z0[1]) + (z0[2] + z0[3])) + ((z0[4] + z0[5]) + (z0[6] + z0[7])), sz1 = ((z1[0] + z1[1]) + (z1[2] + z1[3])) + ((z1[4] + z1[5]) + (z1[6] + z1[7]));
    *(LAS u32x4*)(osc + ((wn * 4 + ks) * 2) * 1024 + lane * 16) = (u32x4){pk2(sa0[0], sa0[1]), pk2(sa0[2], sa0[3]), pk2(sa1[0], sa1[1]), pk2(sa1[2], sa1[3])};
    *(LAS u32x4*)(osc + ((wn * 4 + ks) * 2 + 1) * 1024 + lane * 16) = (u32x4){pk2(sz0[0], sz0[1]), pk2(sz0[2], sz0[3]), pk2(sz1[0], sz1[1]), pk2(sz1[2], sz1[3])};
  }
  STAMP(19);
  float gt[2];
#pragma unroll
  for (int tb = 0; tb < 2; ++tb) gt[tb] = ld_gate(P, 256 * st + tokb + 32 * tb + r);
  LBAR();
  {
    bf16x8 kva[4], za[4];
#pragma unroll
    for (int k = 0; k < 4; ++k) { kva[k] = rd8(osc + ((wn * 4 + k) * 2) * 1024 + lane * 16); za[k] = rd8(osc + ((wn * 4 + k) * 2 + 1) * 1024 + lane * 16); }
    const __amdgpu_buffer_rsrc_t ors = rsrc(P.ws + WS_OT, 4u << 20);
#pragma unroll
    for (int tb = 0; tb < 2; ++tb) {
      f32x16 num = zero, den = zero;
#pragma unroll
      for (int k = 0; k < 4; ++k) { const bf16x8 ph = __builtin_bit_cast(bf16x8, phq[tb][k]); num = MFMA32(kva[k], ph, num); den = MFMA32(za[k], ph, den); }
      const int tok = 256 * st + tokb + 32 * tb + r;
      const float sc = gt[tb] * __builtin_amdgcn_rcpf(den[0] + 1e-6f);
      const unsigned dst = (head * 128 + (tok >> 4)) * 1024 + ((tok & 15) + 16 * h2) * 16;
      st16(ors, dst, (u32x4){pk2(num[0] * sc, num[1] * sc), pk2(num[2] * sc, num[3] * sc), pk2(num[8] * sc, num[9] * sc), pk2(num[10] * sc, num[11] * sc)});
      st16(ors, dst + 512, (u32x4){pk2(num[4] * sc, num[5] * sc), pk2(num[6] * sc, num[7] * sc), pk2(num[12] * sc, num[13] * sc), pk2(num[14] * sc, num[15] * sc)});
    }
  }
  STAMP(20);
  publish(P, FL_O + st * 16 + hp, nonce);
  STAMP(8);
}

DI void fc_role(const Params& P, LAS char* lds, const FcId id, unsigned nonce) {
  using namespace fc;
  const int stl = id.stl, dt = id.dt, c = id.c;
  const int tid = threadIdx.x, lane = tid & 63;
  const int wid = __builtin_amdgcn_readfirstlane(tid >> 6);
  const int g = wid >> 2, w4 = wid & 3, wm = w4 >> 1, wn = w4 & 1;
  const int J0 = c ? 72 : 0;
  const int rr = 4 * w4 + (lane >> 4), cc = lane & 15;
  const unsigned swz = (((cc >> 1) ^ (rr & 7)) << 5) + ((cc & 1) << 4);
  const unsigned voffW2 = rr * 4096 + swz, voffWo = rr * 2048 + swz, voffL = lane * 16;
  const char* wosrc = (const char*)P.wo + dt * 256;
  const char* osrc = P.ws + WS_OT + (size_t)(stl * 8 + 2 * w4) * 1024;
  const char* wptr = (const char*)P.w2 + stl * 256 + (size_t)(J0 + g) * 32 * 4096;
  const char* lptr = P.ws + WS_UT + (size_t)(J0 + g) * 65536 + (size_t)(dt * 8 + 2 * w4) * 1024;
  if (wid == 0) { poll_n(P, FL_U + dt * 16, 16, nonce); }
  BAR();
  STAMP(10);
#pragma unroll
  for (int r = 0; r < 4; ++r) {
    dma16<0, true>(voffL, lptr + (size_t)r * (2 * 65536), (g + 2 * r) * SLOT + 8192 + 2 * w4 * 1024);
    dma16<1024, true>(voffL, lptr + (size_t)r * (2 * 65536), (g + 2 * r) * SLOT + 8192 + 2 * w4 * 1024);
  }
  wptr += 4 * 64 * 4096; lptr += 4 * 2 * 65536;
  unsigned sbi = g * SLOT;
  auto issue1 = [&](auto WO, int i) {
    if constexpr (!WO.value) {
      if (i == 0) dma16<0, false>(voffW2, wptr, sbi + w4 * 1024);
      else if (i == 1) dma16<0, false>(voffW2, wptr + 16 * 4096, sbi + (w4 + 4) * 1024);
      else if (i == 2) dma16<0, true>(voffL, lptr, sbi + 8192 + 2 * w4 * 1024);
      else { dma16<1024, true>(voffL, lptr, sbi + 8192 + 2 * w4 * 1024); wptr += 2 * 32 * 4096; lptr += 2 * 65536; sbi = (sbi + 2 * SLOT) & (NS * SLOT - 1); }
    } else {
      if (i == 0) dma16<0, false>(voffWo, wptr, sbi + w4 * 1024);
      else if (i == 1) dma16<0, false>(voffWo, wptr + 16 * 2048, sbi + (w4 + 4) * 1024);
      else if (i == 2) dma16<0, true>(voffL, lptr, sbi + 8192 + 2 * w4 * 1024);
      else { dma16<1024, true>(voffL, lptr, sbi + 8192 + 2 * w4 * 1024); wptr += 2 * 32 * 2048; lptr += 2 * 131072; sbi = (sbi + 2 * SLOT) & (NS * SLOT - 1); }
    }
  };
  const int kg = lane >> 4, q = (lane & 15) >> 2, p = lane & 3, r7 = 4 * (kg & 1) + q;
  const unsigned base0 = (4 * kg + q) * 256 + (r7 << 5) + 8 * p;
  unsigned offTs[4], offTd[4];
#pragma unroll
  for (int i = 0; i < 4; ++i) { offTs[i] = base0 ^ ((4 * wn + i) << 5); offTd[i] = base0 ^ ((4 * wm + i) << 5); }
  const unsigned offLd = 8192 + 4 * wm * 1024 + lane * 16, offLs = 8192 + 4 * wn * 1024 + lane * 16;
  float og[4];
#pragma unroll
  for (int i = 0; i < 4; ++i) og[i] = 1.0f - ld_gate(P, 128 * stl + 64 * wn + 16 * i + (lane & 15));
  const int srow = 128 * stl + 64 * wn + 16 * (2 * g + c) + (lane & 15);
  const float b2v = bf2f(P.b2[srow]);
  asm volatile("" : "+v"(og[0]), "+v"(og[1]), "+v"(og[2]), "+v"(og[3]));

  f32x4 acc[4][4];
#pragma unroll
  for (int i = 0; i < 4; ++i)
#pragma unroll
    for (int k = 0; k < 4; ++k) acc[i][k] = (f32x4){0.f, 0.f, 0.f, 0.f};

  WAIT_V(0); BAR();
  STAMP(11);
  unsigned sbr = g * SLOT;
  bf16x8 sA[4], dA[4], sB[4], dB[4];
  auto rd1 = [&](auto RK, int i, LAS char* sl, bf16x8 (&sf)[4], bf16x8 (&df)[4]) {
    if constexpr (RK.value == 0) { if (i < 4) df[i] = rd8(sl + offLd + i * 1024); else sf[i - 4] = cat8(trd(sl + offTs[i - 4]), trd(sl + offTs[i - 4] + 4096)); }
    else { if (i < 4) sf[i] = rd8(sl + offLs + i * 1024); else df[i - 4] = cat8(trd(sl + offTd[i - 4]), trd(sl + offTd[i - 4] + 4096)); }
  };
  {
    LAS char* sl = lds + sbr;
#pragma unroll
    for (int i = 0; i < 8; ++i) rd1(IC<0>{}, i, sl, sA, dA);
    sbr = (sbr + 2 * SLOT) & (NS * SLOT - 1);
  }
  auto step = [&](auto RK, auto DK, auto WAITN, bf16x8 (&sc)[4], bf16x8 (&dc)[4], bf16x8 (&sx)[4], bf16x8 (&dx)[4]) {
    if constexpr (WAITN.value >= 0) wait_v<WAITN.value>();
    LBAR();
    LAS char* sl = lds + sbr;
#pragma unroll
    for (int m = 0; m < 16; ++m) {
      mma16(acc[m >> 2][m & 3], dc[m >> 2], sc[m & 3]);
      SB();
      if constexpr (RK.value < 2) { if (m < 8) rd1(RK, m, sl, sx, dx); }
      if constexpr (DK.value < 2) { if (m >= 9 && m < 16 && (m & 1) == 1) issue1(DK, (m - 9) >> 1); }
      SB();
    }
    sbr = (sbr + 2 * SLOT) & (NS * SLOT - 1);
  };
  auto scale = [&]() {
    MFMA_DONE();
#pragma unroll
    for (int i = 0; i < 4; ++i)
#pragma unroll
      for (int k = 0; k < 4; ++k) acc[i][k] *= og[k];
  };
  using I0 = IC<0>; using I1 = IC<1>; using I2 = IC<2>;
  {
    const int n1 = c ? 8 : 16;
    for (int it = 0; it < n1; ++it) { step(I0{}, I0{}, IC<8>{}, sA, dA, sB, dB); step(I0{}, I0{}, IC<8>{}, sB, dB, sA, dA); }
  }
  if (c == 0) {
    for (int it = opaque(1); it > 0; --it) { step(I0{}, I2{}, IC<8>{}, sA, dA, sB, dB); step(I0{}, I2{}, IC<4>{}, sB, dB, sA, dA); }
    for (int it = opaque(1); it > 0; --it) { step(I0{}, I2{}, IC<0>{}, sA, dA, sB, dB); step(I2{}, I2{}, IC<-1>{}, sB, dB, sA, dA); }
    scale();
  } else {
    if (wid == 0) poll_n(P, FL_O + (stl >> 1) * 16, 16, nonce);
    BAR();
    wptr = wosrc + (size_t)g * (32 * 2048); lptr = osrc + (size_t)g * 131072;
    for (int it = opaque(1); it > 0; --it) { step(I0{}, I1{}, IC<8>{}, sA, dA, sB, dB); step(I0{}, I1{}, IC<8>{}, sB, dB, sA, dA); }
    for (int it = opaque(1); it > 0; --it) { step(I0{}, I1{}, IC<8>{}, sA, dA, sB, dB); step(I1{}, I1{}, IC<8>{}, sB, dB, sA, dA); }
    scale();
    for (int it = opaque(6); it > 0; --it) { step(I1{}, I1{}, IC<8>{}, sA, dA, sB, dB); step(I1{}, I1{}, IC<8>{}, sB, dB, sA, dA); }
    for (int it = opaque(1); it > 0; --it) { step(I1{}, I2{}, IC<8>{}, sA, dA, sB, dB); step(I1{}, I2{}, IC<4>{}, sB, dB, sA, dA); }
    for (int it = opaque(1); it > 0; --it) { step(I1{}, I2{}, IC<0>{}, sA, dA, sB, dB); step(I2{}, I2{}, IC<-1>{}, sB, dB, sA, dA); }
  }
  LBAR();
  STAMP(12);
  const int pair = stl * 8 + dt;
  const __amdgpu_buffer_rsrc_t xs = rsrc(P.ws + WS_XCH + (size_t)(pair * 2 + c) * 32768, 32768), xr = rsrc(P.ws + WS_XCH + (size_t)(pair * 2 + (1 - c)) * 32768, 32768);
  f32x4 keep[4];
  auto red = [&](auto G, auto C) {
    constexpr int gg = G.value, cx = C.value;
#pragma unroll
    for (int i = 0; i < 4; ++i)
#pragma unroll
      for (int e = 0; e < 2; ++e) *(LAS f32x4*)(lds + (wid * 8 + i * 2 + e) * 1024 + lane * 16) = acc[i][2 * (1 - gg) + e];
    LBAR();
#pragma unroll
    for (int i = 0; i < 4; ++i) {
      const f32x4 snd = acc[i][2 * gg + (1 - cx)] + *(LAS f32x4*)(lds + ((wid ^ 4) * 8 + i * 2 + (1 - cx)) * 1024 + lane * 16);
      st16(xs, (wid * 4 + i) * 1024 + lane * 16, __builtin_bit_cast(u32x4, snd));
    }
#pragma unroll
    for (int i = 0; i < 4; ++i) keep[i] = acc[i][2 * gg + cx] + *(LAS f32x4*)(lds + ((wid ^ 4) * 8 + i * 2 + cx) * 1024 + lane * 16);
  };
  if (g == 0) { if (c == 0) red(I0{}, I0{}); else red(I0{}, I1{}); }
  else { if (c == 0) red(I1{}, I0{}); else red(I1{}, I1{}); }
  STAMP(13);
  publish(P, FL_X + pair * 2 + c, nonce);
  if (wid == 0) poll_n(P, FL_X + pair * 2 + (1 - c), 1, nonce);
  BAR();
  STAMP(14);
  {
    const float ogv = 1.0f - ld_gate(P, srow);
    const float bias = ogv * b2v;
    u16* orow = P.out + (size_t)srow * D + 128 * dt + 64 * wm + 4 * kg;
    f32x4 o[4];
#pragma unroll
    for (int i = 0; i < 4; ++i) o[i] = ld16(xr, (wid * 4 + i) * 1024 + lane * 16);
#pragma unroll
    for (int i = 0; i < 4; ++i) {
      const f32x4 v = keep[i] + o[i] + bias;
      *(u32x2*)(orow + 16 * i) = (u32x2){pk2(v[0], v[1]), pk2(v[2], v[3])};
    }
  }
  STAMP(15);
}

__global__ __launch_bounds__(512, 2) void k_fused(Params P) {
  extern __shared__ __attribute__((aligned(16))) char smem[];
  LAS char* lds = (LAS char*)smem;
  const int b = blockIdx.x, xcd = b & 7, loc = b >> 3, i = loc & 15;
  const unsigned nonce = (unsigned)__builtin_amdgcn_readfirstlane(__hip_atomic_load(flagp(P, FL_G), RLX_AGENT)) + 1u;
  FcId fid; fid.stl = 4 * (xcd >> 1) + (i & 3); fid.dt = 4 * (xcd & 1) + (i >> 2); fid.c = loc >> 4;
  if (loc < 16) mlp1_role(P, lds, xcd * 16 + i, 4 * (xcd & 1) + (i & 3), 4 * (xcd >> 1) + (i >> 2), fid, nonce);
  else qkv_role(P, lds, 4 * (xcd & 1) + (i & 3), 4 * (xcd >> 1) + (i >> 2), xcd * 16 + i, fid, nonce);
  fc_role(P, lds, fid, nonce);
  if (b == 255 && threadIdx.x == 0) __hip_atomic_store(flagp(P, FL_G), nonce, RLX_AGENT);
}

extern "C" void kernel_launch(void* const* d_in, const int* in_sizes, int n_in, void* d_out, int out_size, void* d_ws, size_t ws_size, hipStream_t stream) {
  Params P;
  P.x = (const u16*)d_in[0]; P.wqkv = (const u16*)d_in[1]; P.wphi = (const u16*)d_in[2]; P.wo = (const u16*)d_in[3];
  P.w1 = (const u16*)d_in[5]; P.b1 = (const u16*)d_in[6]; P.w2 = (const u16*)d_in[7]; P.b2 = (const u16*)d_in[8]; P.gw = (const u16*)d_in[9];
  P.ws = (char*)d_ws; P.out = (u16*)d_out;
  (void)hipFuncSetAttribute((const void*)k_fused, hipFuncAttributeMaxDynamicSharedMemorySize, LDS_BYTES);
  k_fused<<<256, 512, LDS_BYTES, stream>>>(P);
}
```

```cpp
#include <hip/hip_runtime.h>
#include <hip/hip_bf16.h>

typedef unsigned short u16;
typedef short bf16x8 __attribute__((ext_vector_type(8)));
typedef short s16x4 __attribute__((ext_vector_type(4)));
typedef float f32x2 __attribute__((ext_vector_type(2)));
typedef float f32x4 __attribute__((ext_vector_type(4)));
typedef float f32x16 __attribute__((ext_vector_type(16)));
typedef unsigned u32x4 __attribute__((ext_vector_type(4)));
typedef unsigned u32x2 __attribute__((ext_vector_type(2)));
typedef __bf16 bf16x2_t __attribute__((ext_vector_type(2)));
#define LAS __attribute__((address_space(3)))
#define GAS __attribute__((address_space(1)))
#define DI __device__ __forceinline__

constexpr int S = 2048, D = 1024, H = 32, F = 3584;

constexpr size_t WS_FLAGS = 0;
constexpr size_t WS_GATE  = 262144;
constexpr size_t WS_STAT  = 131072;
#define SELFH 0
constexpr size_t WS_HT    = 1u << 20;
#define UOFF 0
constexpr size_t WS_UT    = WS_HT + (4u << 20) + UOFF;
constexpr size_t WS_OT    = WS_HT + (12u << 20);
constexpr size_t WS_KVP   = WS_OT + (4u << 20);
constexpr size_t WS_ZP    = WS_KVP + (2u << 20);
constexpr size_t WS_XCH   = WS_ZP + (1u << 20);
constexpr size_t WS_XFL   = WS_ZP + (512u << 10);
constexpr size_t WS_DBG   = 64u << 20;
constexpr int FL_G = 0, FL_H = 8, FL_KV = FL_H + 128, FL_O = FL_KV + 128, FL_U = FL_O + 128, FL_X = FL_U + 128;
constexpr unsigned SPIN_MAX = 1u << 18;
constexpr int LDS_SINK = 147456 + 1024 + 4096, LDS_BYTES = LDS_SINK + 1024;
constexpr int LDS_WPHI = 147456 + 1024, LDS_CNT = 147456 + 768;

struct Params {
  const u16 *x, *wqkv, *wphi, *wo, *w1, *b1, *w2, *b2, *gw;
  char* ws; u16* out;
};
#define STAMP(n) do {} while (0)

#define STAMPW(n, w) do {} while (0)
DI unsigned umin(unsigned a, unsigned b) { return a < b ? a : b; }
DI unsigned umax(unsigned a, unsigned b) { return a > b ? a : b; }
DI float bf2f(u16 b) { return __uint_as_float(((unsigned)b) << 16); }
DI float bflo(unsigned w) { return __uint_as_float(w << 16); }
DI float bfhi(unsigned w) { return __uint_as_float(w & 0xffff0000u); }
DI unsigned pk2(float a, float b) { bf16x2_t v = {(__bf16)a, (__bf16)b}; return __builtin_bit_cast(unsigned, v); }
DI float dot2(unsigned a, unsigned b, float c) { return __builtin_amdgcn_fdot2_f32_bf16(__builtin_bit_cast(bf16x2_t, a), __builtin_bit_cast(bf16x2_t, b), c, false); }
DI bf16x8 mk8(unsigned a, unsigned b, unsigned c, unsigned d) { u32x4 v = {a, b, c, d}; return __builtin_bit_cast(bf16x8, v); }
DI bf16x8 cat8(s16x4 lo, s16x4 hi) { return __builtin_shufflevector(lo, hi, 0, 1, 2, 3, 4, 5, 6, 7); }

template <int IMM, bool SC1> DI void dma16(unsigned voff, const void* sbase, unsigned ldsaddr) {
  if (SC1) asm volatile("s_mov_b32 m0, %2\n\ts_nop 0\n\tglobal_load_lds_dwordx4 %0, %1 offset:%3 sc1" ::"v"(voff), "s"(sbase), "s"(ldsaddr), "i"(IMM) : "memory");
  else asm volatile("s_mov_b32 m0, %2\n\ts_nop 0\n\tglobal_load_lds_dwordx4 %0, %1 offset:%3" ::"v"(voff), "s"(sbase), "s"(ldsaddr), "i"(IMM) : "memory");
}
template <int OFF, int IMM, bool SC1> DI void dma_first(unsigned voff, const void* sbase, unsigned base) {
  if (SC1) asm volatile("s_add_u32 m0, %2, %3\n\ts_nop 0\n\tglobal_load_lds_dwordx4 %0, %1 offset:%4 sc1" ::"v"(voff), "s"(sbase), "s"(base), "i"(OFF), "i"(IMM) : "memory", "scc");
  else asm volatile("s_add_u32 m0, %2, %3\n\ts_nop 0\n\tglobal_load_lds_dwordx4 %0, %1 offset:%4" ::"v"(voff), "s"(sbase), "s"(base), "i"(OFF), "i"(IMM) : "memory", "scc");
}
template <int IMM, bool SC1> DI void dma_next(unsigned voff, const void* sbase) {
  if (SC1) asm volatile("global_load_lds_dwordx4 %0, %1 offset:%2 sc1" ::"v"(voff), "s"(sbase), "i"(IMM) : "memory");
  else asm volatile("global_load_lds_dwordx4 %0, %1 offset:%2" ::"v"(voff), "s"(sbase), "i"(IMM) : "memory");
}
template <int OFF, int IMM> DI void dma_first_m(unsigned voff, const void* sbase, unsigned base, unsigned long long mask) {
  asm volatile("s_add_u32 m0, %2, %3\n\ts_mov_b64 exec, %5\n\tglobal_load_lds_dwordx4 %0, %1 offset:%4\n\ts_mov_b64 exec, -1" ::"v"(voff), "s"(sbase), "s"(base), "i"(OFF), "i"(IMM), "s"(mask) : "memory", "scc");
}
template <int IMM> DI void dma_next_m(unsigned voff, const void* sbase, unsigned long long mask) {
  asm volatile("s_mov_b64 exec, %3\n\tglobal_load_lds_dwordx4 %0, %1 offset:%2\n\ts_mov_b64 exec, -1" ::"v"(voff), "s"(sbase), "i"(IMM), "s"(mask) : "memory");
}
#define PFMOD ""
DI void pf8k(unsigned voff, const void* sbase) {
  asm volatile("s_mov_b32 m0, %2\n\ts_nop 0\n\tglobal_load_lds_dwordx4 %0, %1 offset:56" PFMOD ::"v"(voff), "s"(sbase), "s"((unsigned)LDS_SINK - 56u) : "memory");
}
DI void pf8k_d(unsigned voff, const void* sbase) {
  asm volatile("s_mov_b32 m0, %2\n\ts_nop 0\n\tglobal_load_lds_dword %0, %1" ::"v"(voff), "s"(sbase), "s"(147456u + 512u) : "memory");
}
DI void word_fetch(const void* sbase, unsigned ldsaddr) {
  asm volatile("s_mov_b32 m0, %2\n\ts_nop 0\n\tglobal_load_lds_dword %0, %1 sc1" ::"v"(0), "s"(sbase), "s"(ldsaddr) : "memory");
}
DI void touch64(const char* p) {
  asm volatile("s_mov_b32 m0, %1\n\ts_nop 0\n\tglobal_load_lds_dword %0, off" ::"v"(p), "s"(147456u + 512u) : "memory");
}
DI const char* uptr(const char* p) {
  const unsigned long long v = (unsigned long long)p;
  return (const char*)(((unsigned long long)(unsigned)__builtin_amdgcn_readfirstlane((int)(v >> 32)) << 32) | (unsigned)__builtin_amdgcn_readfirstlane((int)v));
}
DI void pf_if(unsigned voff, const void* sbase, int off) {
  asm volatile("s_cmp_lg_u32 %3, 0\n\ts_cbranch_scc1 .Lpf_skip%=\n\ts_mov_b32 m0, %2\n\ts_nop 0\n\tglobal_load_lds_dwordx4 %0, %1 offset:56" PFMOD "\n.Lpf_skip%=:" ::"v"(voff), "s"(sbase), "s"((unsigned)LDS_SINK - 56u), "s"(off) : "memory", "scc");
}
DI void wait_4x(int off) {
  asm volatile("s_waitcnt vmcnt(5)\n\ts_cmp_eq_u32 %0, 0\n\ts_cbranch_scc1 .Lw4_skip%=\n\ts_waitcnt vmcnt(4)\n.Lw4_skip%=:" ::"s"(off) : "memory", "scc");
}
template <int N> DI void wait_v() { asm volatile("s_waitcnt vmcnt(%0)" ::"n"(N) : "memory"); }
template <int V> struct IC { static constexpr int value = V; };
#define SB() __builtin_amdgcn_sched_barrier(0)
#define WAIT_V(n) asm volatile("s_waitcnt vmcnt(" #n ")" ::: "memory")
#define WAIT_L0() asm volatile("s_waitcnt lgkmcnt(0)" ::: "memory")
#define BAR() do { asm volatile("s_barrier" ::: "memory"); SB(); } while (0)
template <int N> DI void wait_vl_bar() {
  asm volatile("" ::: "memory");
  __builtin_amdgcn_s_waitcnt(N < 0 ? 0xC07F : ((N & 15) | ((N >> 4) << 14) | 0x0070));
  asm volatile("s_barrier" ::: "memory");
  SB();
}
#define LBAR() do { asm volatile("" ::: "memory"); __builtin_amdgcn_s_waitcnt(0xC07F); asm volatile("s_barrier" ::: "memory"); SB(); } while (0)
DI s16x4 trd(LAS char* p) { return __builtin_amdgcn_ds_read_tr16_b64_v4i16((LAS s16x4*)p); }
DI bf16x8 rd8(LAS char* p) { return *(LAS bf16x8*)p; }
#define MFMA16(a, b, c) __builtin_amdgcn_mfma_f32_16x16x32_bf16((a), (b), (c), 0, 0, 0)
#define MFMA32(a, b, c) __builtin_amdgcn_mfma_f32_32x32x16_bf16((a), (b), (c), 0, 0, 0)

DI void mma16(f32x4& c, const bf16x8& a, const bf16x8& b) { c = MFMA16(a, b, c); }
DI void mma32(f32x16& c, const bf16x8& a, const bf16x8& b) { c = MFMA32(a, b, c); }
#define MFMA_DONE() SB()
DI void mma16t(f32x4& c, const bf16x8& a, const bf16x8& b) { asm volatile("v_mfma_f32_16x16x32_bf16 %0, %1, %2, %0" : "+v"(c) : "v"(a), "v"(b)); }
#define MFMA_SETTLE() asm volatile("s_nop 7\n\ts_nop 7" ::: "memory")
DI int opaque(int n) { asm volatile("" : "+s"(n)); return n; }
template <int CTRL> DI float dpp_mov(float v) { return __builtin_bit_cast(float, __builtin_amdgcn_update_dpp(0, __builtin_bit_cast(int, v), CTRL, 0xF, 0xF, true)); }
DI f32x2 pair32(float v) { const unsigned u = __float_as_uint(v); auto r = __builtin_amdgcn_permlane32_swap(u, u, false, false); return (f32x2){__uint_as_float(r[0]), __uint_as_float(r[1])}; }
DI f32x2 pair16(float v) { const unsigned u = __float_as_uint(v); auto r = __builtin_amdgcn_permlane16_swap(u, u, false, false); return (f32x2){__uint_as_float(r[0]), __uint_as_float(r[1])}; }
DI float wave_sum(float v) {
  v += dpp_mov<0xB1>(v); v += dpp_mov<0x4E>(v); v += dpp_mov<0x141>(v); v += dpp_mov<0x140>(v);
  f32x2 a = pair16(v); v = a.x + a.y;
  f32x2 b = pair32(v); return b.x + b.y;
}

DI void flags_fetch(unsigned lane16, const void* flags, unsigned ldsaddr, int off) {
  unsigned t;
  asm volatile(
      "s_cmp_lg_u32 %4, 0\n\ts_cbranch_scc1 .Lff_skip%=\n\ts_nop 7\n\t"
      "v_lshlrev_b32 %0, 3, %1\n\t"
      "s_mov_b32 m0, %3\n\ts_nop 0\n\t"
      "global_load_lds_dword %0, %2 sc1\n\t"
      "v_add_u32 %0, 0x2000, %0\n\t"
      "s_add_u32 m0, %3, 0x100\n\ts_nop 0\n\t"
      "global_load_lds_dword %0, %2 sc1\n"
      ".Lff_skip%=:"
      : "=&v"(t) : "v"(lane16), "s"(flags), "s"(ldsaddr), "s"(off) : "memory", "scc");
}
DI void flags_check(unsigned lane16, const void* flags, unsigned ldsaddr, unsigned nonce, int off) {
  unsigned a, b, c; unsigned long long m0, m1; int n;
  asm volatile(
      "s_cmp_lg_u32 %10, 0\n\ts_cbranch_scc1 .Lfc_done%=\n\ts_nop 7\n\t"
      "v_lshrrev_b32 %2, 2, %6\n\t"
      "v_add_u32 %2, %8, %2\n\t"
      "ds_read_b32 %0, %2\n\tds_read_b32 %1, %2 offset:256\n\t"
      "s_mov_b32 %5, 0x40000\n\t"
      "s_waitcnt lgkmcnt(0)\n"
      ".Lfc_test%=:\n\t"
      "v_cmp_ne_u32 %3, %9, %0\n\tv_cmp_ne_u32 %4, %9, %1\n\t"
      "s_or_b64 %3, %3, %4\n\t"
      "s_cmp_eq_u64 %3, 0\n\ts_cbranch_scc1 .Lfc_done%=\n\t"
      "s_sub_u32 %5, %5, 1\n\ts_cmp_eq_u32 %5, 0\n\ts_cbranch_scc1 .Lfc_done%=\n\t"
      "v_lshlrev_b32 %2, 3, %6\n\t"
      "global_load_dword %0, %2, %7 sc1\n\t"
      "v_add_u32 %2, 0x2000, %2\n\t"
      "global_load_dword %1, %2, %7 sc1\n\t"
      "s_waitcnt vmcnt(0)\n\t"
      "s_branch .Lfc_test%=\n"
      ".Lfc_done%=:"
      : "=&v"(a), "=&v"(b), "=&v"(c), "=&s"(m0), "=&s"(m1), "=&s"(n)
      : "v"(lane16), "s"(flags), "s"(ldsaddr), "s"(nonce), "s"(off) : "memory", "scc");
}
DI void flags16_fetch(unsigned lane16, const void* flags, unsigned ldsaddr, int off) {
  unsigned t;
  asm volatile(
      "s_cmp_lg_u32 %4, 0\n\ts_cbranch_scc1 .Lgf_skip%=\n\ts_nop 7\n\t"
      "v_and_b32 %0, 0xf0, %1\n\t"
      "v_lshlrev_b32 %0, 3, %0\n\t"
      "s_mov_b32 m0, %3\n\ts_nop 0\n\t"
      "global_load_lds_dword %0, %2 sc1\n"
      ".Lgf_skip%=:"
      : "=&v"(t) : "v"(lane16), "s"(flags), "s"(ldsaddr), "s"(off) : "memory", "scc");
}
DI void flags16_check(unsigned lane16, const void* flags, unsigned ldsaddr, unsigned nonce, int off) {
  unsigned a, c; unsigned long long m0; int n;
  asm volatile(
      "s_cmp_lg_u32 %8, 0\n\ts_cbranch_scc1 .Lgc_done%=\n\ts_nop 7\n\t"
      "v_lshrrev_b32 %1, 2, %4\n\t"
      "v_add_u32 %1, %6, %1\n\t"
      "ds_read_b32 %0, %1\n\t"
      "s_mov_b32 %3, 0x40000\n\t"
      "s_waitcnt lgkmcnt(0)\n"
      ".Lgc_test%=:\n\t"
      "v_cmp_ne_u32 %2, %7, %0\n\t"
      "s_cmp_eq_u64 %2, 0\n\ts_cbranch_scc1 .Lgc_done%=\n\t"
      "s_sub_u32 %3, %3, 1\n\ts_cmp_eq_u32 %3, 0\n\ts_cbranch_scc1 .Lgc_done%=\n\t"
      "v_and_b32 %1, 0xf0, %4\n\t"
      "v_lshlrev_b32 %1, 3, %1\n\t"
      "global_load_dword %0, %1, %5 sc1\n\t"
      "s_waitcnt vmcnt(0)\n\t"
      "s_branch .Lgc_test%=\n"
      ".Lgc_done%=:"
      : "=&v"(a), "=&v"(c), "=&s"(m0), "=&s"(n)
      : "v"(lane16), "s"(flags), "s"(ldsaddr), "s"(nonce), "s"(off) : "memory", "scc");
}
DI void flags32_fetch(unsigned lane16, const void* flags, unsigned ldsaddr, int off) {
  unsigned t;
  asm volatile(
      "s_cmp_lg_u32 %4, 0\n\ts_cbranch_scc1 .Lhf_skip%=\n\ts_nop 7\n\t"
      "v_and_b32 %0, 0x1f0, %1\n\t"
      "v_lshlrev_b32 %0, 3, %0\n\t"
      "s_mov_b32 m0, %3\n\ts_nop 0\n\t"
      "global_load_lds_dword %0, %2 sc1\n"
      ".Lhf_skip%=:"
      : "=&v"(t) : "v"(lane16), "s"(flags), "s"(ldsaddr), "s"(off) : "memory", "scc");
}
DI void flags32_check(unsigned lane16, const void* flags, unsigned ldsaddr, unsigned nonce, int off) {
  unsigned a, c; unsigned long long m0; int n;
  asm volatile(
      "s_cmp_lg_u32 %8, 0\n\ts_cbranch_scc1 .Lhc_done%=\n\ts_nop 7\n\t"
      "v_lshrrev_b32 %1, 2, %4\n\t"
      "v_add_u32 %1, %6, %1\n\t"
      "ds_read_b32 %0, %1\n\t"
      "s_mov_b32 %3, 0x40000\n\t"
      "s_waitcnt lgkmcnt(0)\n"
      ".Lhc_test%=:\n\t"
      "v_cmp_ne_u32 %2, %7, %0\n\t"
      "s_cmp_eq_u64 %2, 0\n\ts_cbranch_scc1 .Lhc_done%=\n\t"
      "s_sub_u32 %3, %3, 1\n\ts_cmp_eq_u32 %3, 0\n\ts_cbranch_scc1 .Lhc_done%=\n\t"
      "v_and_b32 %1, 0x1f0, %4\n\t"
      "v_lshlrev_b32 %1, 3, %1\n\t"
      "global_load_dword %0, %1, %5 sc1\n\t"
      "s_waitcnt vmcnt(0)\n\t"
      "s_branch .Lhc_test%=\n"
      ".Lhc_done%=:"
      : "=&v"(a), "=&v"(c), "=&s"(m0), "=&s"(n)
      : "v"(lane16), "s"(flags), "s"(ldsaddr), "s"(nonce), "s"(off) : "memory", "scc");
}
#define DELAY(n) do {} while (0)
#define RLX_AGENT __ATOMIC_RELAXED, __HIP_MEMORY_SCOPE_AGENT
DI GAS unsigned* flagp(const Params& P, int w) { return (GAS unsigned*)(P.ws + WS_FLAGS) + w * 32; }
DI void publish(const Params& P, int w, unsigned nonce) {
  WAIT_V(0); BAR();
  if (threadIdx.x == 0) __hip_atomic_store(flagp(P, w), nonce, RLX_AGENT);
}
#define POLL_SLEEP 1
DI void poll_n(const Params& P, int w0, int n, unsigned nonce) {
  const int lane = threadIdx.x & 63;
  GAS unsigned* p0 = flagp(P, w0 + (lane < n ? lane : 0));
  GAS unsigned* p1 = flagp(P, w0 + (lane + 64 < n ? lane + 64 : 0));
  for (unsigned spins = 0; spins < SPIN_MAX; ++spins) {
    const unsigned a = __hip_atomic_load(p0, RLX_AGENT), b = __hip_atomic_load(p1, RLX_AGENT);
    if (__all(a == nonce && b == nonce)) break;
    __builtin_amdgcn_s_sleep(POLL_SLEEP);
  }
}
DI void poll_p(const Params& P, int w0, int n, unsigned nonce, unsigned ldsaddr) {
  poll_n(P, w0, n, nonce);
}
DI bool poll_and_peek(const Params& P, int wa, int na, int wb, unsigned nonce) {
  const int lane = threadIdx.x & 63, l = lane & 15;
  GAS unsigned* p = flagp(P, lane & 16 ? wb + l : wa + (l < na ? l : 0));
  unsigned long long ok = 0;
  for (unsigned spins = 0; spins < SPIN_MAX; ++spins) {
    ok = __ballot(__hip_atomic_load(p, RLX_AGENT) == nonce);
    if ((ok & 0xffffull) == 0xffffull) break;
    __builtin_amdgcn_s_sleep(1);
  }
  return (ok & 0xffff0000ull) == 0xffff0000ull;
}
DI float ld_gate(const Params& P, int tok) { return __uint_as_float(__hip_atomic_load((GAS unsigned*)(P.ws + WS_GATE) + tok, RLX_AGENT)); }
DI __amdgpu_buffer_rsrc_t rsrc(const void* p, unsigned bytes) { return __builtin_amdgcn_make_buffer_rsrc((void*)p, 0, bytes, 0x00020000); }
DI void st16(__amdgpu_buffer_rsrc_t r, unsigned off, u32x4 v) { __builtin_amdgcn_raw_buffer_store_b128(v, r, off, 0, 16); }
DI f32x4 ld16(__amdgpu_buffer_rsrc_t r, unsigned off) { return __builtin_bit_cast(f32x4, __builtin_amdgcn_raw_buffer_load_b128(r, off, 0, 16)); }
DI f32x2 ld8(__amdgpu_buffer_rsrc_t r, unsigned off) { return __builtin_bit_cast(f32x2, __builtin_amdgcn_raw_buffer_load_b64(r, off, 0, 16)); }

#define TLBW_LOC 16
#define TLBW_WAVE 6
#define PRIO 0
#define PRIOV 1
#define ENT 6
#define MDX 0
#define QSX 0
DI void tlb_warm(const Params& P, int loc) {
  if (loc == TLBW_LOC && (ENT ? __builtin_amdgcn_readfirstlane(threadIdx.x >> 6) : (int)(threadIdx.x >> 6)) == TLBW_WAVE) {
    const int l = threadIdx.x & 63;
    const char* p;
    if (l < 8) p = (const char*)P.w1 + ((size_t)l << 21) + (l == 7 ? -(1 << 19) : 0) * 0;
    else if (l < 16) p = (const char*)P.w2 + ((size_t)(l - 8) << 21);
    else if (l < 19) p = (const char*)P.wqkv + ((size_t)(l - 16) << 21);
    else if (l < 21) p = (const char*)P.x + ((size_t)(l - 19) << 21);
    else if (l < 22) p = (const char*)P.wo;
    else if (l < 23) p = (const char*)P.b1;
    else if (l < 24) p = (const char*)P.b2;
    else if (l < 25) p = (const char*)P.wphi;
    else if (l < 27) p = (const char*)P.out + ((size_t)(l - 25) << 21);
    else if (l < 47) p = P.ws + ((size_t)(l - 27) << 21);
    else p = (const char*)P.x;
    if (l < 8 && (size_t)l << 21 >= (size_t)S * F * 2) p = (const char*)P.w1;
    if (l >= 8 && l < 16 && (size_t)(l - 8) << 21 >= (size_t)S * F * 2) p = (const char*)P.w2;
    touch64(p);
  }
}
#define TF 0
#define TFG 16
DI void tlb_fine(const Params& P, int part) {
  const int n = part * 64 + (threadIdx.x & 63);
  constexpr int NX = (4 << 20) >> TFG, NQ = (6 << 20) >> TFG, NW = (14 << 20) >> TFG, NO = (2 << 20) >> TFG;
  const char* p = (const char*)P.wo; int k = n - (NX + NQ + 2 * NW);
  if (n < NX + NQ + 2 * NW) { p = (const char*)P.w2; k = n - (NX + NQ + NW); }
  if (n < NX + NQ + NW) { p = (const char*)P.w1; k = n - (NX + NQ); }
  if (n < NX + NQ) { p = (const char*)P.wqkv; k = n - NX; }
  if (n < NX) { p = (const char*)P.x; k = n; }
  if (n >= NX + NQ + 2 * NW + NO) k = 0;
  touch64(p + ((size_t)k << TFG) + (n & 7) * 128);
}
#define EARLY 2
#define NPRE (6 * EARLY)
struct LnPre { u32x4 v[2][2], g0, g1; unsigned gen; };
DI void ln_issue(const Params& P, int m, int skip, LnPre& L) {
  const int lane = threadIdx.x & 63, w = __builtin_amdgcn_readfirstlane(threadIdx.x >> 6);
  const int kb = m >> 1, hf = m & 1;
  const int i0 = 2 * w, tok0 = 32 * kb + (i0 < 8 ? 8 * hf + i0 : 16 + 8 * hf + (i0 - 8));
  const char* r0 = (const char*)(P.x + (size_t)tok0 * D);
  asm volatile(
      "s_cmp_lg_u32 %12, 0\n\ts_cbranch_scc1 .Lli_skip%=\n\t"
      "global_load_dwordx4 %0, %7, %8\n\t"
      "global_load_dwordx4 %1, %7, %8 offset:1024\n\t"
      "global_load_dwordx4 %2, %7, %8 offset:2048\n\t"
      "global_load_dwordx4 %3, %7, %8 offset:3072\n\t"
      "global_load_dwordx4 %4, %7, %9\n\t"
      "global_load_dwordx4 %5, %7, %9 offset:1024\n\t"
      "global_load_dword %6, %10, %11 sc1\n"
      ".Lli_skip%=:"
      : "=&v"(L.v[0][0]), "=&v"(L.v[0][1]), "=&v"(L.v[1][0]), "=&v"(L.v[1][1]), "=&v"(L.g0), "=&v"(L.g1), "=&v"(L.gen)
      : "v"(lane * 16), "s"(r0), "s"(P.gw), "v"(0), "s"(P.ws + WS_FLAGS + FL_G * 128), "s"(skip)
      : "memory", "scc");
}
template <class F, class G, class H> DI unsigned ln_unit(const Params& P, LAS char* lds, int m, F&& after_loads, G&& idle_waves, H&& after_stores, LnPre* pre = nullptr) {
  const int tid = threadIdx.x, lane = tid & 63, w = __builtin_amdgcn_readfirstlane(tid >> 6);
  const int kb = m >> 1, hf = m & 1;
  constexpr int RS = 2048 + 32;
  constexpr int R0 = SELFH ? 49152 : 0, R8 = SELFH ? 73728 : 8 * RS;
  LAS float* gl = (LAS float*)(lds + 147456);
  u32x4 v[2][2], g0, g1; unsigned gen;
  {
    const int i0 = 2 * w, tok0 = 32 * kb + (i0 < 8 ? 8 * hf + i0 : 16 + 8 * hf + (i0 - 8));
    const char* r0 = (const char*)(P.x + (size_t)tok0 * D);
    v[0][0] = pre->v[0][0]; v[0][1] = pre->v[0][1]; v[1][0] = pre->v[1][0]; v[1][1] = pre->v[1][1]; g0 = pre->g0; g1 = pre->g1; gen = pre->gen;
    asm volatile("s_waitcnt vmcnt(0)" : "+v"(v[0][0]), "+v"(v[0][1]), "+v"(v[1][0]), "+v"(v[1][1]), "+v"(g0), "+v"(g1), "+v"(gen) : : "memory");
  }
  STAMP(19);
  after_loads(0);
  float sm[2], sq[2], gd[2];
#pragma unroll
  for (int e = 0; e < 2; ++e) {
    float s0 = 0.f, q0 = 0.f, d0 = 0.f, s1 = 0.f, q1 = 0.f, d1 = 0.f;
#pragma unroll
    for (int j = 0; j < 4; ++j) {
      s0 = dot2(v[e][0][j], 0x3f803f80u, s0); q0 = dot2(v[e][0][j], v[e][0][j], q0); d0 = dot2(v[e][0][j], g0[j], d0);
      s1 = dot2(v[e][1][j], 0x3f803f80u, s1); q1 = dot2(v[e][1][j], v[e][1][j], q1); d1 = dot2(v[e][1][j], g1[j], d1);
    }
    sm[e] = s0 + s1; sq[e] = q0 + q1; gd[e] = d0 + d1;
    after_loads(1 + e);
  }
#pragma unroll
  for (int e = 0; e < 2; ++e) { sm[e] = wave_sum(sm[e]); sq[e] = wave_sum(sq[e]); gd[e] = wave_sum(gd[e]); if (e == 0) after_loads(3); }
  float rstd_[2], nm_[2];
#pragma unroll
  for (int e = 0; e < 2; ++e) {
    const float mean = sm[e] * (1.0f / D);
    const float var = fmaxf(sq[e] * (1.0f / D) - mean * mean, 0.f);
    rstd_[e] = __builtin_amdgcn_rsqf(var + 1e-5f); nm_[e] = -mean * rstd_[e];
  }
  after_loads(4);
#pragma unroll
  for (int e = 0; e < 2; ++e) {
    const int i = 2 * w + e;
    const float rstd = rstd_[e], nm = nm_[e];
#pragma unroll
    for (int c = 0; c < 2; ++c) {
      u32x4 o;
#pragma unroll
      for (int j = 0; j < 4; ++j) o[j] = pk2(__builtin_fmaf(bflo(v[e][c][j]), rstd, nm), __builtin_fmaf(bfhi(v[e][c][j]), rstd, nm));
      *(LAS u32x4*)(lds + R0 + (i >> 3) * R8 + (i & 7) * RS + c * 1024 + lane * 16) = o;
    }
    if (lane == 0) gl[i] = __builtin_amdgcn_rcpf(1.0f + __builtin_amdgcn_exp2f(-1.4426950408889634f * gd[e]));
    if (e == 0) after_loads(5);
  }
  LBAR();
  STAMP(20);
  if (w < 4) {
    if (tid < 16) {
      const int tok = 32 * kb + (tid < 8 ? 8 * hf + tid : 16 + 8 * hf + (tid - 8));
      __hip_atomic_store((GAS unsigned*)(P.ws + WS_GATE) + tok, __float_as_uint(gl[tid]), RLX_AGENT);
    }
    const int e = (lane >> 4) & 1, q = (lane & 15) >> 2, p = lane & 3, db0 = 16 * w + (lane >> 5);
    LAS char* src = lds + R0 + (4 * e + q) * RS + db0 * 32 + 8 * p;
    const __amdgpu_buffer_rsrc_t rs = rsrc(P.ws + WS_HT, 4u << 20);
    const unsigned dst = (kb * 64 + db0) * 1024 + (2 * hf + e) * 256 + (lane & 15) * 16;
#pragma unroll
    for (int n = 0; n < 8; ++n)
      st16(rs, dst + n * 2048, __builtin_bit_cast(u32x4, cat8(trd(src + n * 64), trd(src + R8 + n * 64))));
    after_stores();
  } else idle_waves((unsigned)__builtin_amdgcn_readfirstlane(gen) + 1u);
  return (unsigned)__builtin_amdgcn_readfirstlane(gen) + 1u;
}


namespace fc { constexpr int SLOT = 16384, NS = 8; }
#define NA4 9
constexpr int FC_J0 = 8 * NA4;
#define OUT_AUX 16
#define CODE_PF 7
#define FC_TIED 0
#define ASY 0
#define FV 0
constexpr int FC_RD[6][12] = {
  {0, 1, 2, 3, 4, 4, 5, 5, 6, 6, 7, 7},
  {0, 1, 2, 3, 4, 5, 6, 7, 8, 9, 10, 11},
  {8, 9, 10, 11, 0, 1, 2, 3, 4, 5, 6, 7},
  {0, 2, 4, 6, 1, 3, 5, 7, 8, 9, 10, 11},
  {1, 2, 3, 4, 5, 6, 7, 8, 9, 10, 11, 12},
  {0, 1, 2, 3, 4, 4, 6, 6, 8, 8, 10, 10}};
constexpr int FC_DM[10][4] = {{9, 11, 13, 15}, {12, 13, 14, 15}, {1, 5, 9, 13}, {12, 13, 14, 15}, {0, 13, 14, 15}, {5, 7, 9, 11}, {0, 2, 13, 15}, {1, 3, 11, 14}, {4, 6, 13, 15}, {8, 9, 13, 15}};
#define FRD 0
#define FDM 0
constexpr int fc_rdpos(int i) { return FV == 0 ? i : FV == 1 ? (3 * i) / 2 : FV == 2 ? (3 * i) / 2 + 1 : FV == 3 ? i : i; }
constexpr int fc_dmapos(int i) { return FV == 0 ? 9 + 2 * i : FV == 1 ? 2 + 4 * i - (i == 3) * 0 - 0 : FV == 2 ? 4 * i : FV == 3 ? 3 + 4 * i : 9 + 2 * i; }
#define ROT 2
#define ASH -4
#define PXSL 0
#define FCEP 1
#define RROT 0
DI int wrap112(int s) { return s >= 112 ? s - 112 : s; }
struct FcId { int stl, dt, c, j0, recv, s0, own; };
DI int fc_rot(const FcId id) { return RROT ? (id.j0 & 4) >> 1 : ROT; }
DI int fc_slice(const FcId id, int n) { const int s = id.s0 + n; return wrap112(s >= id.own ? s + 6 : s); }
DI void fc_prologue_w(const Params& P, const FcId id) {
  const int lane = threadIdx.x & 63, wid = __builtin_amdgcn_readfirstlane(threadIdx.x >> 6), g = wid >> 2, w4 = wid & 3;
  const int rr = 4 * w4 + (lane >> 4), cc = lane & 15;
  const unsigned voffW2 = rr * 4096 + (((cc >> 1) ^ (rr & 7)) << 5) + ((cc & 1) << 4);
#define WOFF 0
  const unsigned mb = g * 65536 + w4 * 4096;
  const char* src = (const char*)P.w2 + (id.stl < 12 ? WOFF : 0) + id.stl * 256 + (size_t)((id.c ? id.j0 : 0) + g) * 32 * 4096;
#pragma unroll
  for (int r = 0; r < 4; ++r) {
    dma16<0, false>(voffW2, src + (size_t)r * (64 * 4096), mb + ((r + fc_rot(id)) & 3) * 16384);
    dma16<0, false>(voffW2, src + (size_t)r * (64 * 4096) + 16 * 4096, mb + ((r + fc_rot(id)) & 3) * 16384 + 1024);
  }
}

DI void fc_prologue_u(const Params& P, const FcId id) {
  const int lane = threadIdx.x & 63, wid = __builtin_amdgcn_readfirstlane(threadIdx.x >> 6), g = wid >> 2, w4 = wid & 3;
  const unsigned mb = g * 65536 + w4 * 4096;
  const char* lptr = uptr(P.ws + WS_UT + (size_t)((id.c ? id.j0 : 0) + g) * 65536 + (size_t)(id.dt * 8 + 2 * w4) * 1024);
#pragma unroll
  for (int r = 0; r < 4; ++r) {
    dma16<0, true>(lane * 16, lptr + (size_t)r * (2 * 65536), mb + ((r + fc_rot(id)) & 3) * 16384 + 2048);
    dma16<1024, true>(lane * 16, lptr + (size_t)r * (2 * 65536), mb + ((r + fc_rot(id)) & 3) * 16384 + 2048);
  }
}
DI int fc_first_flag(const FcId id) { return FL_U + id.dt * 16 + (id.c ? id.j0 : 0) / 7; }
DI int fc_first_count(const FcId id) { const int j = id.c ? id.j0 : 0; return (j + 13 + 2 * fc_rot(id)) / 7 - j / 7 + 1; }
struct FcG { float og[4]; unsigned b2v, b2w; };
DI FcG fc_gates(const Params& P, const FcId id) {
  const int lane = threadIdx.x & 63, wid = __builtin_amdgcn_readfirstlane(threadIdx.x >> 6), g = wid >> 2, wn = wid & 1;
  FcG r;
#pragma unroll
  for (int i = 0; i < 4; ++i) r.og[i] = ld_gate(P, 128 * id.stl + 64 * wn + 16 * (FCEP ? (i + 2 * g) & 3 : i) + (lane & 15));
  const int srow = 128 * id.stl + 64 * wn + 32 * g + (lane & 15);
  r.b2v = P.b2[srow]; r.b2w = P.b2[srow + 16];
  return r;
}
struct Role { unsigned nonce; int mine, all, late; FcG fg; };

#define PF16 6
#define PFSK16 1
DI unsigned pf_lanes(int lane, int jq) {
  return PFSK16 == 1 ? (lane * 128 + ((jq + lane) & 127)) * 128
       : PFSK16 == 2 ? (((jq & 3) * 64 + lane) * 32 + (jq >> 2)) * 128
       : PFSK16 == 3 ? (((jq & 3) * 64 + lane) * 32 + (((jq >> 2) + lane) & 31)) * 128
       : PFSK16 == 4 ? ((lane >> 1) * 256 + (lane & 1) + 2 * jq) * 128
       : PFSK16 == 5 ? ((lane >> 2) * 512 + (lane & 3) + 4 * jq) * 128
       : lane * 128 + jq * 8192;
}
#define PF6SK 0
#define M1L2 0
namespace m1 { constexpr int SLOT = 24576, NS = 6; }
#define M1EP 3
#define M1T 0
#define M1V 1
#define PFV 4
#define PT_M 0xffffffffull
#define PT_N 0xffffffffffffffffull
#define M1SLEEP 6
#define M1E ((unsigned)(PT_M) != 0xffffffffu)
#define M1L ((unsigned long long)(PT_N) != 0xffffffffffffffffull)
DI const char* pf_src(const Params& P, int te, int piece) {
  const int bf = te >> 4;
  const char* base = bf == 0 ? (const char*)P.wqkv : bf == 1 ? (const char*)P.w1 : bf == 2 ? (const char*)P.w2 : (const char*)P.wo;
  return uptr(base + ((size_t)(te & 15) << 20) + (size_t)piece * 8192);
}
constexpr int m1_rdpos(int i) {
  const int g = 0;
  if (M1V == 0) return i;
  if (M1V == 1) return 2 * i;
  if (M1V == 2) return 2 * i;
  if (M1V == 3) return 2 * i + 1;
  if (M1V == 4) return i;
  if (M1V == 10) return g == 0 ? i : 14 + i;
  if (M1V == 11) return g == 0 ? 2 * i : 2 * i + 1;
  if (M1V == 12) return g == 0 ? i : 13 + i;
  if (M1V == 13) return g == 0 ? 2 * i : 2 * i + 1;
  return i;
}
constexpr int m1_dmapos(int i) {
  const int g = 0;
  if (M1V == 0) return 12 + 2 * i;
  if (M1V == 1) return 1 + 4 * i;
  if (M1V == 2) return 3 + 4 * i + (i > 2);
  if (M1V == 3) return 4 * i + (i > 2) * 2;
  if (M1V == 4) return 11 + 3 * i;
  if (M1V == 10) return g == 0 ? 14 + 2 * i : 2 * i;
  if (M1V == 11) return g == 0 ? 1 + 4 * i : 4 + 4 * i;
  if (M1V == 12) return g == 0 ? 12 + 3 * i - (i > 3) : 2 * i + 1;
  if (M1V == 13) return g == 0 ? 1 + 5 * i - (i > 2) * 1 : 3 + 5 * i - (i > 1) * 1;
  return 12 + 2 * i;
}
DI f32x2 gelu2(f32x2 x) {
  constexpr float C0 = -2.885390081777927f * 0.7978845608028654f, C1 = C0 * 0.044715f;
  const f32x2 t = x * x * C1 + C0;
  const f32x2 a = x * t;
  f32x2 e = {__builtin_amdgcn_exp2f(a.x), __builtin_amdgcn_exp2f(a.y)};
  e = e + 1.0f;
  const f32x2 r = {__builtin_amdgcn_rcpf(e.x), __builtin_amdgcn_rcpf(e.y)};
  return x * r;
}
DI Role mlp1_role(const Params& P, LAS char* lds, int m, int dt, int ft, const FcId fid, LnPre* pre = nullptr) {
  using namespace m1;
  const int tid = threadIdx.x, lane = tid & 63;
  const int wid = __builtin_amdgcn_readfirstlane(tid >> 6);
  const int g = wid >> 2, w4 = wid & 3, wm = w4 >> 1, wn = w4 & 1, odd = ft & 1;
  const char* wptr = (const char*)P.w1 + (224 * ft - 32 * odd) * 2 + (size_t)g * (32 * 7168);
  const char* hptr = P.ws + WS_HT + (size_t)(dt * 8 + 2 * w4) * 1024 + (size_t)g * 65536;
  const int r0 = 2 * w4 + (lane >> 5), c = lane & 31;
  const unsigned voffW = r0 * 7168 + (((c >> 1) ^ r0) << 5) + ((c & 1) << 4);
  const unsigned voffH = lane * 16;
  unsigned mb = g * (3 * SLOT) + w4 * 6144;
  unsigned voffWi[4];
#pragma unroll
  for (int i = 0; i < 4; ++i) voffWi[i] = voffW + i * (8 * 7168) + 3072 - 1024 * i;
  STAMP(0);
  DELAY(2);
  u32x2 bias[7];
  const unsigned nonce = ln_unit(P, lds, m, [&](int k) {
    if (k != 0) return;
    if (wid == 7 && (m & 15) < 8) {
      unsigned long long pc; unsigned back, len;
      asm volatile("s_getpc_b64 %0\n.Lk_second:\n\ts_mov_b32 %1, .Lk_second-.Lk_first\n\ts_mov_b32 %2, .Lk_last-.Lk_first" : "=s"(pc), "=s"(back), "=s"(len));
      const unsigned off = (m & 15) * 8192 + lane * 128 + 64;
      pf8k_d(off < len ? off : 0u, (const void*)(pc - back));
    }
    if (g == 1) pf8k(PF6SK ? pf_lanes(lane, m) : lane * 128 + m * 8192, uptr(w4 < 2 ? (const char*)P.w1 + (size_t)w4 * (1u << 20) : (const char*)P.wqkv + (size_t)(w4 - 2) * (1u << 20)));
    if ((M1E || TF == 1) && g == 1) {
#pragma unroll
      for (int i = 0; i < 7; ++i) bias[i] = (u32x2){0u, 0u};
    } else {
#pragma unroll
    for (int i = 0; i < 7; ++i) bias[i] = *(const u32x2*)(P.b1 + 224 * ft + 4 * (lane >> 4) + 16 * (7 * wn + i));
    }
  }, [&](unsigned) {
    if (TF == 1) {
      constexpr int NP = (((4 + 6 + 14 + 14 + 2) << 20) >> TFG) / 64;
      const int part = (m & 15) * 4 + w4;
      if (part < NP) tlb_fine(P, part);
    }
    if (M1E) {
      const int te = (int)((unsigned)(PT_M) >> (w4 * 8)) & 0xff;
      __builtin_amdgcn_s_sleep(M1SLEEP);
      if (te != 0xff) pf8k(lane * 128, pf_src(P, te, m));
    }
  }, [&]() {}, pre);
  STAMP(1);
  if (!(M1E || PFV == 5 || TF == 1) || g == 0) {
    asm volatile("" : "+v"(bias[0]), "+v"(bias[1]), "+v"(bias[2]), "+v"(bias[3]), "+v"(bias[4]), "+v"(bias[5]), "+v"(bias[6]));
    WAIT_V(0);
  }
  BAR();
  STAMP(7);
  if (tid == 0) __hip_atomic_store(flagp(P, FL_H + m), nonce, RLX_AGENT);
  auto stageW = [&](int r) {
#pragma unroll
    for (int i = 0; i < 4; ++i) dma16<0, false>(voffW, wptr + (size_t)r * (64 * 7168) + i * (8 * 7168), mb + r * SLOT + i * 1024);
  };
  auto stageH = [&](int r) {
    dma16<0, true>(voffH, hptr + (size_t)r * (2 * 65536), mb + r * SLOT + 4096);
    dma16<1024, true>(voffH, hptr + (size_t)r * (2 * 65536), mb + r * SLOT + 4096);
  };
  stageW(0); stageW(1); stageW(2);
  if (wid == 0) poll_p(P, FL_H, 32, nonce, LDS_WPHI + 512);
  BAR();
  STAMP(2);
  stageH(0); stageH(1); stageH(2);
  wptr += 3 * 64 * 7168; hptr += 3 * 2 * 65536 - 1024;
  const unsigned mbc = mb + 3072;
  auto issue1 = [&](auto J, int i) {
    constexpr int OFF = J.value * SLOT;
    if (i == 0) dma_first<OFF, -3072, false>(voffWi[0], wptr, mbc);
    else if (i == 1) dma_next<-2048, false>(voffWi[1], wptr);
    else if (i == 2) dma_next<-1024, false>(voffWi[2], wptr);
    else if (i == 3) { dma_next<0, false>(voffWi[3], wptr); wptr += 2 * 32 * 7168; }
    else if (i == 4) dma_next<1024, true>(voffH, hptr);
    else { dma_next<2048, true>(voffH, hptr); hptr += 2 * 65536; }
  };
  const int kg = lane >> 4, q = (lane & 15) >> 2, p = lane & 3, r7 = 4 * (kg & 1) + q;
  const unsigned base0 = g * (3 * SLOT) + (2 * (kg & 1) + (q >> 1)) * 6144 + (kg >> 1) * 1024 + (q & 1) * 512 + (r7 << 5) + 8 * p;
  const int u0 = 7 * wn + 2 * odd;
  unsigned offW[7];
#pragma unroll
  for (int i = 0; i < 7; ++i) offW[i] = base0 ^ ((u0 + i) << 5);
  const unsigned offH = g * (3 * SLOT) + 2 * wm * 6144 + 4096 + lane * 16;
  const unsigned offH0 = offH + g * 6144, offH1 = offH + (1 - g) * 6144;

  f32x4 acc[7][4];
  {
    const unsigned keepb = g == 0 ? 0xffffffffu : 0u;
#pragma unroll
    for (int i = 0; i < 7; ++i) {
      const unsigned b0 = bias[i][0] & keepb, b1w = bias[i][1] & keepb;
#pragma unroll
      for (int d = 0; d < 4; ++d) acc[i][d] = (f32x4){bflo(b0), bfhi(b0), bflo(b1w), bfhi(b1w)};
    }
  }

  WAIT_V(0);
  BAR();
  STAMP(3);
  if (PRIO & 1) { if (g == 0) __builtin_amdgcn_s_setprio(PRIOV); }
  bf16x8 wA[7], hA[4], wB[7], hB[4];
  auto rd1 = [&](auto J, int i, bf16x8 (&wf)[7], bf16x8 (&hf)[4]) {
    LAS char* sl = lds + J.value * SLOT;
    if (i < 4) hf[i] = rd8(sl + (i < 2 ? offH0 : offH1) + (i & 1) * 1024);
    else wf[i - 4] = cat8(trd(sl + offW[i - 4]), trd(sl + offW[i - 4] + 2048));
  };
#pragma unroll
  for (int i = 0; i < 11; ++i) rd1(IC<0>{}, i, wA, hA);
  const char* hflags = uptr(P.ws + WS_FLAGS + FL_H * 128);
  const __amdgpu_buffer_rsrc_t xclr = rsrc(P.ws + WS_XCH + (size_t)(ft * 8 + dt) * 32768, 32768);
  auto step = [&](auto J, auto READ, auto ISSUE, auto WAITN, bf16x8 (&wc)[7], bf16x8 (&hc)[4], bf16x8 (&wx)[7], bf16x8 (&hx)[4], int hook = 0, int off = 1) {
    if (hook == 2) { wait_v<WAITN.value < 0 ? 0 : WAITN.value>(); flags_check(voffH, hflags, LDS_WPHI, nonce, off); }
    wait_vl_bar<WAITN.value>();
    if (hook == 1) flags_fetch(voffH, hflags, LDS_WPHI, off);
    if (hook == 4) flags32_fetch(voffH, hflags, LDS_WPHI, off);
    if constexpr (ISSUE.value == 3) {
      constexpr int JP = (J.value + 2) % 3;
      dma16<0, true>(voffH, hptr + 1024, mb + JP * SLOT + 4096); dma16<1024, true>(voffH, hptr + 1024, mb + JP * SLOT + 4096);
      hptr += 2 * 65536;
    }
    if (hook == 3 && M1L) {
      const int te = (int)((unsigned long long)(PT_N) >> (wid * 8)) & 0xff;
      if (te != 0xff) pf8k(lane * 128, pf_src(P, te, m));
    }
#pragma unroll
    for (int mm = 0; mm < 28; ++mm) {
#define M1ORD 0
      if (hook == 6) {
        const int row = (mm < 14 ? mm : mm - 14) >> 1, col = (mm < 14 ? 2 : 0) + (mm & 1);
        mma16(acc[row][col], wc[row], hc[col]);
        SB();
#pragma unroll
        for (int k = 0; k < 14; ++k)
          if ((k + 3 > 2 * k + 1 ? k + 3 : 2 * k + 1) == mm) *(LAS f32x4*)(lds + (wid * 15 + (k & 1) * 7 + (k >> 1)) * 1024 + lane * 16) = acc[k >> 1][2 + (k & 1)];
        SB();
        continue;
      }
      if (M1T) mma16t(acc[mm >> 2][mm & 3], wc[mm >> 2], hc[mm & 3]);
      else if (M1ORD == 0) mma16(acc[mm >> 2][mm & 3], wc[mm >> 2], hc[mm & 3]);
      else mma16(acc[mm % 7][mm / 7], wc[mm % 7], hc[mm / 7]);
      SB();
      if constexpr (READ.value) {
#pragma unroll
        for (int i = 0; i < 11; ++i) if (m1_rdpos(i) == mm) rd1(IC<(J.value + 1) % 3>{}, i, wx, hx);
      }
      if constexpr (ISSUE.value) {
#pragma unroll
        for (int i = 0; i < (ISSUE.value == 2 ? 4 : 6); ++i) if (m1_dmapos(i) == mm) issue1(J, i);
        if (M1L2 && mm == 24) issue1(J, 6);
      }
      if (hook == 3 && (mm & 7) == 1) st16(xclr, (wid * 4 + (mm >> 3)) * 1024 + lane * 16, (u32x4){~0u, ~0u, ~0u, ~0u});
      SB();
    }
  };
  using J0 = IC<0>; using J1 = IC<1>; using J2 = IC<2>; using Y = IC<1>; using N = IC<0>; using W6 = IC<M1L2 ? 8 : 6>;
  for (int it = 0; it < 4; ++it) {
    const int off = __builtin_amdgcn_readfirstlane(wid | it);
    step(J0{}, Y{}, Y{}, W6{}, wA, hA, wB, hB); step(J1{}, Y{}, Y{}, W6{}, wB, hB, wA, hA); step(J2{}, Y{}, Y{}, W6{}, wA, hA, wB, hB);
    step(J0{}, Y{}, Y{}, W6{}, wB, hB, wA, hA, 1, off); step(J1{}, Y{}, Y{}, W6{}, wA, hA, wB, hB); step(J2{}, Y{}, Y{}, W6{}, wB, hB, wA, hA, 2, off);
  }
  { int it = opaque(1); do {
    step(J0{}, Y{}, Y{}, W6{}, wA, hA, wB, hB); step(J1{}, Y{}, Y{}, W6{}, wB, hB, wA, hA); step(J2{}, Y{}, Y{}, W6{}, wA, hA, wB, hB);
    step(J0{}, Y{}, Y{}, W6{}, wB, hB, wA, hA); step(J1{}, Y{}, Y{}, W6{}, wA, hA, wB, hB); step(J2{}, Y{}, N{}, W6{}, wB, hB, wA, hA);
  } while (--it > 0); }
  { int it = opaque(1); do { step(J0{}, Y{}, N{}, IC<0>{}, wA, hA, wB, hB, 3); step(J1{}, N{}, N{}, IC<-1>{}, wB, hB, wA, hA, M1EP == 3 ? 6 : 0); } while (--it > 0); }
  MFMA_DONE();
  STAMP(4);
  if (PRIO & 1) __builtin_amdgcn_s_setprio(0);
  DELAY(4);
  auto xaddr = [&](int w, int e) { return lds + (w * 15 + e) * 1024 + lane * 16; };
  const int wP = wid ^ 4, wQ0 = wid ^ 1, wQ1 = wid ^ 5;
  const __amdgpu_buffer_rsrc_t urs = rsrc(P.ws + WS_UT, 8u << 20);
  const unsigned utile = (7 * ft) * 65536 + (dt * 8 + 4 * wm + 2 * g) * 1024 + lane * 16;
  FcG fg = fc_gates(P, fid);
  auto epi = [&](auto G, auto WN) {
    constexpr int gg = G.value, wnn = WN.value, IM = wnn ? 0 : 6, IMQ = wnn ? 6 : 0;
    *(LAS f32x4*)xaddr(wid, 14) = acc[IM][1 - wnn];
    LBAR();
    STAMP(16);
    f32x4 t[2][7], tq;
#pragma unroll
    for (int dd = 0; dd < 2; ++dd)
#pragma unroll
      for (int i = 0; i < 7; ++i)
        if (i != IM || dd == wnn) t[dd][i] = acc[i][(M1EP == 3 ? 0 : 2 * gg) + dd] + *(LAS f32x4*)xaddr(wP, dd * 7 + i);
    tq = *(LAS f32x4*)xaddr(wQ0, 14) + *(LAS f32x4*)xaddr(wQ1, wnn * 7 + IMQ);
    auto fin = [&](f32x4 v) -> u32x2 {
      const f32x2 a = gelu2((f32x2){v[0], v[1]}), b = gelu2((f32x2){v[2], v[3]});
      return (u32x2){pk2(a.x, a.y), pk2(b.x, b.y)};
    };
#pragma unroll
    for (int dd = 0; dd < 2; ++dd) {
#pragma unroll
      for (int pr = 0; pr < 3; ++pr) {
        const int i0 = 2 * pr + wnn, b = 7 * wnn + i0;
        const u32x2 lo = fin(t[dd][i0]), hi = fin(t[dd][i0 + 1]);
        st16(urs, utile + (b >> 1) * 65536 + dd * 1024, (u32x4){lo[0], lo[1], hi[0], hi[1]});
      }
    }
    {
      const u32x2 mine = fin(t[wnn][IM]), oth = fin(tq);
      const u32x4 odd4 = wnn ? (u32x4){oth[0], oth[1], mine[0], mine[1]} : (u32x4){mine[0], mine[1], oth[0], oth[1]};
      st16(urs, utile + 3 * 65536 + wnn * 1024, odd4);
    }
  };
  if (wn == 0) epi(IC<0>{}, IC<0>{}); else epi(IC<0>{}, IC<1>{});
  STAMP(5);
  STAMPW(21, 4); STAMPW(22, 7); STAMPW(26, 3);
  publish(P, FL_U + dt * 16 + ft, nonce);
  STAMP(6);
  fc_prologue_w(P, fid);
  return Role{nonce, 0, 0, 0, fg};
}

namespace qk { constexpr int XS = 32768, WB = 98304, WS = 24576, NT = 16; }
#define QV 0
constexpr int qk_wpos(int i) { return QV == 0 ? 2 * i : QV == 1 ? 4 * i : QV == 2 ? i : QV == 3 ? 3 * i + 1: 10 - 0 * i; }
constexpr int qk_xpos(int i) { return QV == 0 ? 3 * i : QV == 1 ? 3 * i + 1 : QV == 2 ? i : QV == 3 ? 2 * i + 4 : 0; }
DI float max2(float a, float b) { return __builtin_amdgcn_fmed3f(a, b, __builtin_inff()); }
DI f32x2 swap32(float a, float b) { auto r = __builtin_amdgcn_permlane32_swap(__float_as_uint(a), __float_as_uint(b), false, false); return (f32x2){__uint_as_float(r[0]), __uint_as_float(r[1])}; }
DI void softmax_pm(const f32x16& pq, u32x4 (&out)[4]) {
  float mx = fmaxf(pq[0], pq[1]), nm = fmaxf(-pq[0], -pq[1]);
#pragma unroll
  for (int i = 2; i < 16; i += 2) { mx = fmaxf(fmaxf(mx, pq[i]), pq[i + 1]); nm = fmaxf(fmaxf(nm, -pq[i]), -pq[i + 1]); }
  {
    const f32x2 a = swap32(mx, nm);
    const float m = max2(a.x, a.y);
    const f32x2 b = swap32(m, m);
    mx = b.x; nm = b.y;
  }
  constexpr float L2E = 1.4426950408889634f;
  const float mxs = -mx * L2E, mns = -nm * L2E;
  f32x2 ep[8], en[8], sp, sn;
#pragma unroll
  for (int i = 0; i < 8; ++i) {
    const f32x2 pv = {pq[2 * i], pq[2 * i + 1]};
    const f32x2 a = pv * L2E + mxs, b = mns - pv * L2E;
    ep[i] = (f32x2){__builtin_amdgcn_exp2f(a.x), __builtin_amdgcn_exp2f(a.y)};
    en[i] = (f32x2){__builtin_amdgcn_exp2f(b.x), __builtin_amdgcn_exp2f(b.y)};
    if (i == 0) { sp = ep[0]; sn = en[0]; } else { sp += ep[i]; sn += en[i]; }
  }
  float rp, rn;
  {
    const f32x2 a = swap32(sp.x + sp.y, sn.x + sn.y);
    const float rc = __builtin_amdgcn_rcpf(a.x + a.y);
    const f32x2 b = swap32(rc, rc);
    rp = b.x; rn = b.y;
  }
#pragma unroll
  for (int s = 0; s < 2; ++s)
#pragma unroll
    for (int j = 0; j < 4; ++j) {
      const f32x2 a = ep[4 * s + j] * rp, b = en[4 * s + j] * rn;
      out[s][j] = pk2(a.x, a.y); out[2 + s][j] = pk2(b.x, b.y);
    }
}
DI bf16x8 pack8(const f32x16& x, int s) {
  return mk8(pk2(x[8 * s], x[8 * s + 1]), pk2(x[8 * s + 2], x[8 * s + 3]), pk2(x[8 * s + 4], x[8 * s + 5]), pk2(x[8 * s + 6], x[8 * s + 7]));
}
DI Role qkv_role(const Params& P, LAS char* lds, int st, int hp, int jq, const FcId fid) {
  using namespace qk;
  const int tid = threadIdx.x, lane = tid & 63;
  const int wid = __builtin_amdgcn_readfirstlane(tid >> 6);
  const int g = wid >> 2, w4 = wid & 3, wm2 = w4 >> 1, wn = w4 & 1;
  const int head = 2 * hp + wn, tokb = 128 * g + 64 * wm2;
  const int h2 = lane >> 5, r = lane & 31;
  unsigned voffW[3];
#pragma unroll
  for (int b = 0; b < 3; ++b) {
    const int li = 8 * b + (lane >> 3), row = li / 3, part = li % 3;
    voffW[b] = row * 6144 + part * 2048 + (((lane & 7) ^ (((row >> 1) & 1) << 2)) << 4);
  }
  const unsigned voffX = (lane >> 3) * 2048 + (((lane & 7) ^ ((4 * (wid & 1) + (lane >> 4)) & 7)) << 4);
  const char* wsrc = (const char*)P.wqkv + hp * 128;
  const char* xsrc = (const char*)P.x + (size_t)(256 * st) * 2048;
  unsigned voffXi[4];
#pragma unroll
  for (int i = 0; i < 4; ++i) voffXi[i] = voffX + i * (8 * 16384 - 1024);
#pragma unroll
  for (int i = 0; i < 3; ++i) voffW[i] -= 1024 * i;
  const char* wp = wsrc + (size_t)(8 * wid) * 6144;
  const char* xp = xsrc + (size_t)wid * 16384;
  const unsigned mW = WB + wid * 3072, mX = wid * 4096;
  auto issueW = [&](unsigned sb, int i) {
    if (i == 0) dma_first<0, 0, false>(voffW[0], wp, mW + sb);
    else if (i == 1) dma_next<1024, false>(voffW[1], wp);
    else { dma_next<2048, false>(voffW[2], wp); wp += 64 * 6144; }
  };
  auto issueX = [&](unsigned sb, int i) {
    if (i == 0) dma_first<0, 0, false>(voffXi[0], xp, mX + sb);
    else if (i == 1) dma_next<1024, false>(voffXi[1], xp);
    else if (i == 2) dma_next<2048, false>(voffXi[2], xp);
    else { dma_next<3072, false>(voffXi[3], xp); xp += 128; }
  };
  STAMP(0);
  DELAY(1);
#pragma unroll
  for (int i = 0; i < 4; ++i) issueX(0, i);
#pragma unroll
  for (int i = 0; i < 3; ++i) issueW(0, i);
  BAR();
  dma16<0, false>(lane * 16, (const char*)P.wphi + hp * 4096 + (wid & 3) * 1024, LDS_WPHI + (wid & 3) * 1024);
#pragma unroll
  for (int i = 0; i < 3; ++i) issueW(WS, i);
#pragma unroll
  for (int i = 0; i < 4; ++i) issueX(XS, i);
#define PFSK 0
  const unsigned pfl = PFSK ? (lane * 128 + ((jq + PFSK * lane) & 127)) * 128 : lane * 128;
  const unsigned jqo = PFSK ? 0 : jq * 8192;
#define PFLATE 0
  if (!PFLATE && wid < 4) pf8k(pfl, (wid < 2 ? (const char*)P.wqkv + (size_t)wid * (1u << 20) : (const char*)P.w1 + (size_t)(wid - 2) * (1u << 20)) + (size_t)jqo);
  unsigned offX[4];
#pragma unroll
  for (int s = 0; s < 4; ++s) offX[s] = (r >> 3) * 4096 + (tokb >> 6) * 1024 + (r & 7) * 128 + (((2 * s + h2) ^ ((r >> 1) & 7)) << 4);
  const int blk = (lane >> 4) & 1, q = (lane & 15) >> 2, p = lane & 3;
  const unsigned offW = WB + (8 * h2 + q) * 384 + 64 * (wn ^ (q >> 1)) + 32 * blk + 8 * p;

  f32x16 aq[2], ak[2], av[2];
#pragma unroll
  for (int t = 0; t < 2; ++t)
#pragma unroll
    for (int i = 0; i < 16; ++i) { aq[t][i] = 0.f; ak[t][i] = 0.f; av[t][i] = 0.f; }

  WAIT_V(8);
  BAR();
  STAMP(3);
  if (PFLATE == 1 && wid < 4) pf8k(pfl, (wid < 2 ? (const char*)P.wqkv + (size_t)wid * (1u << 20) : (const char*)P.w1 + (size_t)(wid - 2) * (1u << 20)) + (size_t)jqo);
  bf16x8 xA[2][2], wA[3][2], xB[2][2], wB[3][2];
  auto rd1 = [&](int i, LAS char* sx, LAS char* sw, int hf, bf16x8 (&xf)[2][2], bf16x8 (&wf)[3][2]) {
    if (i < 4) xf[i & 1][i >> 1] = rd8(sx + offX[2 * hf + (i >> 1)] + (i & 1) * 16384);
    else { const int part = (i - 4) % 3, s2 = (i - 4) / 3, s = 2 * hf + s2; wf[part][s2] = cat8(trd(sw + offW + (16 * s) * 384 + part * 128), trd(sw + offW + (16 * s + 4) * 384 + part * 128)); }
  };
#pragma unroll
  for (int i = 0; i < 10; ++i) rd1(i, lds, lds, 0, xA, wA);
  auto half = [&](auto READ, auto ISSUE, LAS char* sx, LAS char* sw, int hf, unsigned sb, const char* pfp, int pfo, bf16x8 (&xc)[2][2], bf16x8 (&wc)[3][2], bf16x8 (&xx)[2][2], bf16x8 (&wx)[3][2]) {
#pragma unroll
    for (int m = 0; m < 12; ++m) {
      const int s2 = m / 6, tb = (m % 6) / 3, part = m % 3;
      if (part == 0) mma32(aq[tb], wc[0][s2], xc[tb][s2]);
      else if (part == 1) mma32(ak[tb], wc[1][s2], xc[tb][s2]);
      else mma32(av[tb], xc[tb][s2], wc[2][s2]);
      SB();
      if constexpr (READ.value) { if (m < 10) rd1(m, sx, sw, hf, xx, wx); }
      if constexpr (ISSUE.value == 1) {
#pragma unroll
        for (int i = 0; i < 3; ++i) if (qk_wpos(i) == m) issueW(sb, i);
        if (m == 6) pf_if(pfl, uptr(pfp), __builtin_amdgcn_readfirstlane(pfo));
      }
      if constexpr (ISSUE.value == 2) {
#pragma unroll
        for (int i = 0; i < 4; ++i) if (qk_xpos(i) == m) issueX(sb, i);
      }
      SB();
    }
  };
  const char* pfw1 = (const char*)P.w1 + (2u << 20) + (size_t)jqo;
  const long long pfd0 = ((const char*)P.wqkv - (const char*)P.w1) + (2ll << 20), pfd2 = ((const char*)P.w2 - (const char*)P.w1) - (14ll << 20),
                  pfd3 = ((const char*)P.wo - (const char*)P.w2) - (14ll << 20);
  unsigned xa = 0, xb = XS, xc2 = 2 * XS;
  int pfoff = wid >> 2;
  for (int t = 0; t < NT - 2; ++t) {
    half(IC<1>{}, IC<2>{}, lds + xa, lds + (t & 1) * WS, 1, xc2, nullptr, 0, xA, wA, xB, wB);
    wait_4x(__builtin_amdgcn_readfirstlane(pfoff));
    LBAR();
    const int n = 2 * t + (wid & 1);
    pfoff = ((wid >> 1) ^ (t & 3)) | ((t + 4) >> 4);
    const unsigned long long tw = n < 8 ? 0x0416151403131202ull : n < 16 ? 0x1d1c1b1a05191817ull : 0x2726252423222120ull;
    const int te = (int)(tw >> ((n & 7) * 8)) & 0xff, tb_ = te >> 4;
    const char* pfp = pfw1 - (2ll << 20) + ((long long)(te & 15) << 20) + ((pfd0 - (2ll << 20)) & (long long)((tb_ - 1) >> 31)) + ((pfd2 + (14ll << 20)) & (long long)((1 - tb_) >> 31));
    half(IC<1>{}, IC<1>{}, lds + xb, lds + ((t + 1) & 1) * WS, 0, (t & 1) * WS, pfp, pfoff, xB, wB, xA, wA);
    const unsigned x0 = xa; xa = xb; xb = xc2; xc2 = x0;
  }
  { int it = opaque(1); do {
    half(IC<1>{}, IC<0>{}, lds + 2 * XS, lds, 1, 0, nullptr, 0, xA, wA, xB, wB);
    WAIT_V(0); LBAR();
    pf8k(pfl, (wid < 6 ? (const char*)P.w2 + (size_t)(8 + wid) * (1u << 20) : (const char*)P.wo + (size_t)(wid - 6) * (1u << 20)) + (size_t)jqo);
    half(IC<1>{}, IC<0>{}, lds, lds + WS, 0, 0, nullptr, 0, xB, wB, xA, wA);
  } while (--it > 0); }
  { int it = opaque(1); do {
    half(IC<1>{}, IC<0>{}, lds, lds + WS, 1, 0, nullptr, 0, xA, wA, xB, wB);
    half(IC<0>{}, IC<0>{}, lds, lds, 0, 0, nullptr, 0, xB, wB, xA, wA);
  } while (--it > 0); }
  MFMA_DONE();
  LBAR();
  STAMP(4);
  DELAY(3);
  const unsigned gen = __hip_atomic_load(flagp(P, FL_G), RLX_AGENT);
#define PF_G() do {} while (0)
  const char* hflags = uptr(P.ws + WS_FLAGS + FL_H * 128);
  flags_fetch(lane * 16, hflags, 139264, wid);
  PF_G();
  bf16x8 wphi[2];
#pragma unroll
  for (int s = 0; s < 2; ++s) {
    LAS char* a = lds + LDS_WPHI + wn * 2048 + (16 * s + 4 * h2 + q) * 64 + blk * 32 + p * 8;
    wphi[s] = cat8(trd(a), trd(a + 8 * 64));
  }
  LAS char* img = lds + wid * 8192;
  f32x16 zero;
#pragma unroll
  for (int i = 0; i < 16; ++i) zero[i] = 0.f;
  const unsigned sw = (r & 1) | (((r >> 2) & 3) << 1);
#pragma unroll
  for (int tb = 0; tb < 2; ++tb) {
    f32x16 pk = MFMA32(wphi[0], pack8(ak[tb], 0), zero); pk = MFMA32(wphi[1], pack8(ak[tb], 1), pk);
    u32x4 ph[4]; softmax_pm(pk, ph);
#pragma unroll
    for (int fb = 0; fb < 2; ++fb)
#pragma unroll
      for (int g4 = 0; g4 < 4; ++g4) {
        const u32x2 w = {ph[2 * fb + (g4 >> 1)][2 * (g4 & 1)], ph[2 * fb + (g4 >> 1)][2 * (g4 & 1) + 1]};
        *(LAS u32x2*)(img + (32 * tb + r) * 128 + 64 * (fb ^ ((r >> 1) & 1)) + 8 * ((2 * g4 + h2) ^ sw)) = w;
      }
  }
  WAIT_L0();
  STAMP(16);
  f32x16 kv[2] = {zero, zero}, zz[2] = {zero, zero};
  const bf16x8 ones = mk8(0x3f803f80u, 0x3f803f80u, 0x3f803f80u, 0x3f803f80u);
#pragma unroll
  for (int tb = 0; tb < 2; ++tb)
#pragma unroll
    for (int s = 0; s < 2; ++s) {
      const bf16x8 vb = pack8(av[tb], s);
#pragma unroll
      for (int fb = 0; fb < 2; ++fb) {
        s16x4 a[2];
#pragma unroll
        for (int t = 0; t < 2; ++t) {
          const int row = 16 * s + 8 * t + 4 * h2 + q;
          const unsigned swr = (row & 1) | (((row >> 2) & 3) << 1);
          a[t] = trd(img + (32 * tb + row) * 128 + 64 * (fb ^ ((row >> 1) & 1)) + 8 * ((4 * blk + p) ^ swr));
        }
        const bf16x8 af = cat8(a[0], a[1]);
        kv[fb] = MFMA32(af, vb, kv[fb]); zz[fb] = MFMA32(af, ones, zz[fb]);
      }
    }
  STAMP(17);
  if (wid == 0) { WAIT_V(0); flags_check(lane * 16, hflags, 139264, (unsigned)__builtin_amdgcn_readfirstlane(gen) + 1u, 0); }
  LAS char* red = lds + 65536;
  constexpr int RS = 8448;
#pragma unroll
  for (int fb = 0; fb < 2; ++fb) {
#pragma unroll
    for (int c = 0; c < 4; ++c) *(LAS f32x4*)(red + wid * RS + (fb * 4 + c) * 1024 + lane * 16) = (f32x4){kv[fb][4 * c], kv[fb][4 * c + 1], kv[fb][4 * c + 2], kv[fb][4 * c + 3]};
    if (r == 0) {
#pragma unroll
      for (int c = 0; c < 4; ++c) *(LAS f32x4*)(red + wid * RS + 8192 + (fb * 2 + h2) * 64 + c * 16) = (f32x4){zz[fb][4 * c], zz[fb][4 * c + 1], zz[fb][4 * c + 2], zz[fb][4 * c + 3]};
    }
  }
  LBAR();
  float gt[2];
#pragma unroll
  for (int tb = 0; tb < 2; ++tb) gt[tb] = ld_gate(P, 256 * st + tokb + 32 * tb + r);
  const int qd = 2 * g + wm2;
  const __amdgpu_buffer_rsrc_t kvrs = rsrc(P.ws + WS_KVP, 2u << 20), zrs = rsrc(P.ws + WS_ZP, 65536);
  {
    f32x4 s2[2];
#pragma unroll
    for (int e = 0; e < 2; ++e) {
      const int ch = 2 * qd + e;
      f32x4 s = (f32x4){0.f, 0.f, 0.f, 0.f};
#pragma unroll
      for (int w = 0; w < 4; ++w) s += *(LAS f32x4*)(red + (2 * w + wn) * RS + ch * 1024 + lane * 16);
      s2[e] = s;
    }
    st16(kvrs, (st * 32 + head) * 4096 + qd * 1024 + lane * 16, (u32x4){pk2(s2[0][0], s2[0][1]), pk2(s2[0][2], s2[0][3]), pk2(s2[1][0], s2[1][1]), pk2(s2[1][2], s2[1][3])});
    if (qd == 0 && lane < 16) {
      f32x4 s = (f32x4){0.f, 0.f, 0.f, 0.f};
#pragma unroll
      for (int w = 0; w < 4; ++w) s += *(LAS f32x4*)(red + (2 * w + wn) * RS + 8192 + lane * 16);
      st16(zrs, ((st * 32 + head) * 64 + lane * 4) * 4, __builtin_bit_cast(u32x4, s));
    }
  }
  WAIT_L0();
  STAMP(18);
  const unsigned nonce = (unsigned)__builtin_amdgcn_readfirstlane(gen) + 1u;
  publish(P, FL_KV + hp * 8 + st, nonce);
  asm volatile("" : "+v"(gt[0]), "+v"(gt[1]));
  STAMP(5);
  fc_prologue_w(P, fid);
  const FcG fg = fc_gates(P, fid);
  u32x4 phq[2][4];
#pragma unroll
  for (int tb = 0; tb < 2; ++tb) {
    f32x16 pq = MFMA32(wphi[0], pack8(aq[tb], 0), zero); pq = MFMA32(wphi[1], pack8(aq[tb], 1), pq);
    softmax_pm(pq, phq[tb]);
  }
  STAMP(6);
  if (wid == 0) poll_p(P, FL_KV + hp * 8, 8, nonce, LDS_WPHI + 512);
  BAR();
  STAMP(7);
  LAS char* osc = lds + 131072;
  {
    const int ks = qd, fb = ks >> 1, s = ks & 1;
    const f32x2 zp = ld8(zrs, ((lane >> 2) & 7) * (32 * 256) + (head * 64 + (fb * 2 + h2) * 16 + 8 * s + 2 * (lane & 3)) * 4);
    u32x4 a[8];
#pragma unroll
    for (int t = 0; t < 8; ++t) a[t] = __builtin_bit_cast(u32x4, ld16(kvrs, head * 4096 + ks * 1024 + lane * 16 + t * (32 * 4096)));
    float zx = zp.x, zy = zp.y;
    zx += dpp_mov<0x124>(zx); zy += dpp_mov<0x124>(zy);
    zx += dpp_mov<0x128>(zx); zy += dpp_mov<0x128>(zy);
    { const f32x2 u = pair16(zx), v = pair16(zy); zx = u.x + u.y; zy = v.x + v.y; }
    if ((lane & 28) == 0) *(LAS unsigned*)(osc + 8192 + (wn * 4 + ks) * 32 + h2 * 16 + (lane & 3) * 4) = pk2(zx, zy);
    float sv[8];
    unsigned clo = 0x00003f80u, chi = 0x3f800000u;
    asm volatile("" : "+s"(clo), "+s"(chi));
#pragma unroll
    for (int j = 0; j < 4; ++j) {
      float lo = bflo(a[0][j]), hi = bfhi(a[0][j]);
#pragma unroll
      for (int t = 1; t < 8; ++t) { lo = dot2(a[t][j], clo, lo); hi = dot2(a[t][j], chi, hi); }
      sv[2 * j] = lo; sv[2 * j + 1] = hi;
    }
    *(LAS u32x4*)(osc + (wn * 4 + ks) * 1024 + lane * 16) = (u32x4){pk2(sv[0], sv[1]), pk2(sv[2], sv[3]), pk2(sv[4], sv[5]), pk2(sv[6], sv[7])};
  }
  STAMP(19);
  const unsigned ulds = LDS_WPHI + 2048 + wid * 256;
  const char* uflags0 = uptr(P.ws + WS_FLAGS + (size_t)fc_first_flag(fid) * 128);
  {
    const int nf = fc_first_count(fid), l = lane & 3;
    asm volatile("s_mov_b32 m0, %2\n\ts_nop 0\n\tglobal_load_lds_dword %0, %1 sc1" ::"v"((l < nf ? l : 0) * 128), "s"(uflags0), "s"(ulds) : "memory");
  }
  int mine = 0;
  LBAR();
  {
    bf16x8 kva[4], za[4];
#pragma unroll
    for (int k = 0; k < 4; ++k) { kva[k] = rd8(osc + (wn * 4 + k) * 1024 + lane * 16); za[k] = rd8(osc + 8192 + (wn * 4 + k) * 32 + h2 * 16); }
    const __amdgpu_buffer_rsrc_t ors = rsrc(P.ws + WS_OT, 4u << 20);
#pragma unroll
    for (int tb = 0; tb < 2; ++tb) {
      f32x16 num = zero, den = zero;
#pragma unroll
      for (int k = 0; k < 4; ++k) { const bf16x8 ph = __builtin_bit_cast(bf16x8, phq[tb][k]); num = MFMA32(kva[k], ph, num); den = MFMA32(za[k], ph, den); }
      if (tb == 1) {
        WAIT_V(2);
        mine = __all(*(LAS unsigned*)(lds + ulds + lane * 4) == nonce);
        if (mine) fc_prologue_u(P, fid);
        if (lane == 0) *(LAS unsigned*)(lds + LDS_CNT + 32 + wid * 4) = mine;
      }
      const int tok = 256 * st + tokb + 32 * tb + r;
      const float sc = gt[tb] * __builtin_amdgcn_rcpf(den[0] + 1e-6f);
      const unsigned dst = (head * 128 + (tok >> 4)) * 1024 + ((tok & 15) + 16 * h2) * 16;
      st16(ors, dst, (u32x4){pk2(num[0] * sc, num[1] * sc), pk2(num[2] * sc, num[3] * sc), pk2(num[8] * sc, num[9] * sc), pk2(num[10] * sc, num[11] * sc)});
      st16(ors, dst + 512, (u32x4){pk2(num[4] * sc, num[5] * sc), pk2(num[6] * sc, num[7] * sc), pk2(num[12] * sc, num[13] * sc), pk2(num[14] * sc, num[15] * sc)});
    }
  }
  STAMP(20);
  LBAR();
  const int all = __all(*(LAS unsigned*)(lds + LDS_CNT + 32 + (lane & 7) * 4) != 0u);
  STAMP(8);
  return Role{nonce, __builtin_amdgcn_readfirstlane(mine), __builtin_amdgcn_readfirstlane(all), FL_O + st * 16 + hp, fg};
}


DI float vmax(float a, float b) { float r; asm("v_max_f32 %0, %1, %2\n\ts_nop 1" : "=v"(r) : "v"(a), "v"(b)); return r; }
DI float vmaxneg(float a, float b) { float r; asm("v_max_f32 %0, -%1, -%2\n\ts_nop 1" : "=v"(r) : "v"(a), "v"(b)); return r; }
DI f32x2 swap16(float a, float b) { auto r = __builtin_amdgcn_permlane16_swap(__float_as_uint(a), __float_as_uint(b), false, false); return (f32x2){__uint_as_float(r[0]), __uint_as_float(r[1])}; }
DI void softmax_pm2(const f32x4 (&pa)[2], const f32x4 (&pb)[2], u32x4& ap, u32x4& an, u32x4& bp, u32x4& bn) {
  float mx[2], nm[2];
#pragma unroll
  for (int e = 0; e < 2; ++e) {
    const f32x4 (&v)[2] = e ? pb : pa;
    mx[e] = vmax(fmaxf(fmaxf(v[0][3], v[1][0]), v[1][1]), fmaxf(fmaxf(fmaxf(fmaxf(v[0][0], v[0][1]), v[0][2]), v[1][2]), v[1][3]));
    nm[e] = vmaxneg(fminf(fminf(v[0][3], v[1][0]), v[1][1]), fminf(fminf(fminf(fminf(v[0][0], v[0][1]), v[0][2]), v[1][2]), v[1][3]));
  }
  {
    const f32x2 a = swap16(mx[0], mx[1]), b = swap16(nm[0], nm[1]);
    const f32x2 c = swap32(vmax(a.x, a.y), vmax(b.x, b.y));
    const float t = vmax(c.x, c.y);
    const f32x2 d = swap32(t, t);
    const f32x2 e = swap16(d.x, d.x), f = swap16(d.y, d.y);
    mx[0] = e.x; mx[1] = e.y; nm[0] = f.x; nm[1] = f.y;
  }
  constexpr float L2E = 1.4426950408889634f;
  f32x2 ep[2][4], en[2][4]; float sp[2], sn[2];
#pragma unroll
  for (int e = 0; e < 2; ++e) {
    const f32x4 (&v)[2] = e ? pb : pa;
    const float mxs = -mx[e] * L2E, mns = -nm[e] * L2E;
#pragma unroll
    for (int i = 0; i < 4; ++i) {
      const f32x2 pv = {v[i >> 1][2 * (i & 1)], v[i >> 1][2 * (i & 1) + 1]};
      const f32x2 a = pv * L2E + mxs, b = mns - pv * L2E;
      ep[e][i] = (f32x2){__builtin_amdgcn_exp2f(a.x), __builtin_amdgcn_exp2f(a.y)};
      en[e][i] = (f32x2){__builtin_amdgcn_exp2f(b.x), __builtin_amdgcn_exp2f(b.y)};
    }
    const f32x2 s = (ep[e][0] + ep[e][1]) + (ep[e][2] + ep[e][3]), n = (en[e][0] + en[e][1]) + (en[e][2] + en[e][3]);
    sp[e] = s.x + s.y; sn[e] = n.x + n.y;
  }
  {
    const f32x2 a = swap16(sp[0], sp[1]), b = swap16(sn[0], sn[1]);
    const f32x2 c = swap32(a.x + a.y, b.x + b.y);
    const float t = __builtin_amdgcn_rcpf(c.x + c.y);
    const f32x2 d = swap32(t, t);
    const f32x2 e = swap16(d.x, d.x), f = swap16(d.y, d.y);
    sp[0] = e.x; sp[1] = e.y; sn[0] = f.x; sn[1] = f.y;
  }
#pragma unroll
  for (int i = 0; i < 4; ++i) {
    const f32x2 a = ep[0][i] * sp[0], b = en[0][i] * sn[0], c = ep[1][i] * sp[1], d = en[1][i] * sn[1];
    ap[i] = pk2(a.x, a.y); an[i] = pk2(b.x, b.y); bp[i] = pk2(c.x, c.y); bn[i] = pk2(d.x, d.y);
  }
}
DI bf16x8 pack44(const f32x4& a, const f32x4& b) { return mk8(pk2(a[0], a[1]), pk2(a[2], a[3]), pk2(b[0], b[1]), pk2(b[2], b[3])); }
#define QRV 0
constexpr int Q_RD[4][10] = {{0, 1, 2, 3, 4, 6, 8, 10, 12, 14}, {0, 1, 2, 3, 5, 8, 11, 14, 17, 20}, {0, 2, 4, 6, 8, 10, 12, 14, 16, 18}, {4, 5, 6, 7, 0, 2, 9, 11, 13, 15}};
#define QDV 0
constexpr int Q_DW[3][4] = {{0, 4, 8, 12}, {1, 5, 9, 13}, {15, 17, 19, 21}};
constexpr int Q_DX[3][4] = {{0, 6, 12, 18}, {1, 7, 13, 19}, {15, 17, 19, 21}};
DI void qkv_first(const Params& P, int st, int hp, int skip) {
  using namespace qk;
  const int lane = threadIdx.x & 63, wid = __builtin_amdgcn_readfirstlane(threadIdx.x >> 6);
  const unsigned voffX = (lane >> 3) * 2048 + (((lane & 7) ^ ((4 * (wid & 1) + (lane >> 4)) & 7)) << 4);
  const char* xp = (const char*)P.x + (size_t)(256 * st + 8 * wid) * 2048;
  const unsigned mW = WB + wid * 3072, mX = wid * 4096;
  asm volatile(
      "s_cmp_lg_u32 %6, 0\n\ts_cbranch_scc1 .Lqx_skip%=\n\t"
      "s_mov_b32 m0, %5\n\ts_nop 0\n\t"
      "global_load_lds_dwordx4 %0, %4\n\t"
      "global_load_lds_dwordx4 %1, %4 offset:1024\n\t"
      "global_load_lds_dwordx4 %2, %4 offset:2048\n\t"
      "global_load_lds_dwordx4 %3, %4 offset:3072\n"
      ".Lqx_skip%=:"
      ::"v"(voffX), "v"(voffX + (8 * 16384 - 1024)), "v"(voffX + 2 * (8 * 16384 - 1024)), "v"(voffX + 3 * (8 * 16384 - 1024)), "s"(xp), "s"(mX), "s"(skip) : "memory", "scc");
  const char* wp = (const char*)P.wqkv + hp * 128 + (size_t)(8 * wid) * 6144;
  unsigned voffW[3];
#pragma unroll
  for (int b = 0; b < 3; ++b) {
    const int li = 8 * b + (lane >> 3), row = li / 3, part = li % 3;
    voffW[b] = row * 6144 + part * 2048 + (((lane & 7) ^ (((row >> 1) & 1) << 2) ^ ((wid & 1) << 1)) << 4) - 1024 * b;
  }
  asm volatile(
      "s_cmp_lg_u32 %5, 0\n\ts_cbranch_scc1 .Lqw_skip%=\n\t"
      "s_mov_b32 m0, %4\n\ts_nop 0\n\t"
      "global_load_lds_dwordx4 %0, %3\n\t"
      "global_load_lds_dwordx4 %1, %3 offset:1024\n\t"
      "global_load_lds_dwordx4 %2, %3 offset:2048\n"
      ".Lqw_skip%=:"
      ::"v"(voffW[0]), "v"(voffW[1]), "v"(voffW[2]), "s"(wp), "s"(mW), "s"(skip) : "memory", "scc");
}
DI Role qkv_role16(const Params& P, LAS char* lds, int st, int hp, int jq, const FcId fid) {
  using namespace qk;
  const int tid = threadIdx.x, lane = tid & 63;
  const int wid = __builtin_amdgcn_readfirstlane(tid >> 6);
  const int g = wid >> 2, w4 = wid & 3, wm2 = w4 >> 1, wn = w4 & 1;
  const int head = 2 * hp + wn, tokb = 128 * g + 64 * wm2;
  const int kg = lane >> 4, l15 = lane & 15, q = l15 >> 2, p4 = lane & 3;
  unsigned voffW[3];
#pragma unroll
  for (int b = 0; b < 3; ++b) {
    const int li = 8 * b + (lane >> 3), row = li / 3, part = li % 3;
    voffW[b] = row * 6144 + part * 2048 + (((lane & 7) ^ (((row >> 1) & 1) << 2) ^ ((wid & 1) << 1)) << 4);
  }
  const unsigned voffX = (lane >> 3) * 2048 + (((lane & 7) ^ ((4 * (wid & 1) + (lane >> 4)) & 7)) << 4);
  const char* wsrc = (const char*)P.wqkv + hp * 128;
  const char* xsrc = (const char*)P.x + (size_t)(256 * st) * 2048;
  unsigned voffXi[4];
#pragma unroll
  for (int i = 0; i < 4; ++i) voffXi[i] = voffX + i * (8 * 16384 - 1024);
#pragma unroll
  for (int i = 0; i < 3; ++i) voffW[i] -= 1024 * i;
#define QKR 0
  const char* wp = wsrc + (size_t)(8 * wid) * 6144 + (size_t)QKR * 64 * 6144;
  const char* xp = xsrc + (size_t)wid * 16384 + QKR * 128;
  int tw_ = QKR, tx_ = QKR;
  const unsigned mW = WB + wid * 3072, mX = wid * 4096;
  auto issueW = [&](unsigned sb, int i) {
    if (i == 0) dma_first<0, 0, false>(voffW[0], wp, mW + sb);
    else if (i == 1) dma_next<1024, false>(voffW[1], wp);
    else { dma_next<2048, false>(voffW[2], wp); wp += 64 * 6144; if (QKR) { ++tw_; if (tw_ == 16) wp -= 1024 * 6144; } }
  };
  auto issueX = [&](unsigned sb, int i) {
    if (i == 0) dma_first<0, 0, false>(voffXi[0], xp, mX + sb);
    else if (i == 1) dma_next<1024, false>(voffXi[1], xp);
    else if (i == 2) dma_next<2048, false>(voffXi[2], xp);
    else { dma_next<3072, false>(voffXi[3], xp); xp += 128; if (QKR) { ++tx_; if (tx_ == 16) xp -= 2048; } }
  };
  STAMP(0);
  DELAY(1);
  wp += 64 * 6144; xp += 128;
  BAR();
#define WPL 1
  if (!WPL) dma16<0, false>(lane * 16, (const char*)P.wphi + hp * 4096 + (wid & 3) * 1024, LDS_WPHI + (wid & 3) * 1024);
#pragma unroll
  for (int i = 0; i < 3; ++i) issueW(WS, i);
#pragma unroll
  for (int i = 0; i < 4; ++i) issueX(XS, i);
  const unsigned pfl = pf_lanes(lane, jq);
  const char* pf0 = (wid < 2 ? (const char*)P.wqkv + (size_t)wid * (1u << 20) : (const char*)P.w1 + (size_t)(wid - 2) * (1u << 20));
#define QPF 0
  if (PF16 == 0 && wid < 4) pf8k(pfl, pf0);
  if ((PF16 == 2 || PF16 == 6) && wid < 4 && !WPL) pf8k(0, P.wphi);
  if (PF16 == 3 && wid < 4) { if (wid == 0 || wid == 2) pf8k(pfl, pf0); }
  if (PF16 == 7 && wid < 4) { if (wid < 2) pf8k(pfl, pf0); else pf8k(0, P.wphi); }
  if (PF16 == 8 && wid < 4) { if (wid >= 2) pf8k(pfl, pf0); else pf8k(0, P.wphi); }
  unsigned offX[2], offW[2];
#pragma unroll
  for (int h = 0; h < 2; ++h) {
    offX[h] = (l15 >> 3) * 4096 + (tokb >> 6) * 1024 + (l15 & 7) * 128 + (((4 * h + kg) ^ (l15 >> 1)) << 4);
    offW[h] = WB + (8 * kg + q) * 384 + 64 * (wn ^ (q >> 1)) + 32 * (h ^ (kg & 1)) + 8 * p4;
  }
#define QT0 0x0416151403131202ull
#define QT1 0x1d1c1b1a05191817ull
#define QT2 0x2726252423222120ull
#define QT14 0x31302d2c2b2a2928ull
#define QT4 0xffffffffffffffffull
  auto pfent = [&](unsigned long long tw) { return (int)(tw >> (wid * 8)) & 0xff; };
  auto pfadr = [&](int te) {
    const int bf = te >> 4;
    return uptr((bf == 0 ? (const char*)P.wqkv : bf == 1 ? (const char*)P.w1 : bf == 2 ? (const char*)P.w2 : (const char*)P.wo) + ((size_t)(te == 0xff ? 0 : te & 15) << 20));
  };
  const char* pfq[3]; int pfn[3];
#pragma unroll
  for (int i = 0; i < 3; ++i) {
    const int te = pfent(i == 0 ? QT0 : i == 1 ? QT1 : QT2);
    pfq[i] = pfadr(te); pfn[i] = te == 0xff;
  }
  f32x4 aq[2][4], ak[2][4], av[4][2];
#pragma unroll
  for (int a = 0; a < 2; ++a)
#pragma unroll
    for (int b = 0; b < 4; ++b) { aq[a][b] = (f32x4){0.f, 0.f, 0.f, 0.f}; ak[a][b] = (f32x4){0.f, 0.f, 0.f, 0.f}; av[b][a] = (f32x4){0.f, 0.f, 0.f, 0.f}; }

  if (QPF) { if (wid < 2) WAIT_V(8); else WAIT_V(7); } else
  if (WPL) { if (PF16 == 0 && wid < 4) WAIT_V(8); else WAIT_V(7); } else
  if (PF16 == 3) { if (wid == 0 || wid == 2) WAIT_V(9); else WAIT_V(8); } else
  WAIT_V(8);
  BAR();
  STAMP(3);
  if (PF16 == 1 && wid < 4) pf8k(pfl, pf0);
  if (PF16 == 3 && (wid == 1 || wid == 3)) pf8k(pfl, pf0);
  if (PRIO & 2) { if (g == 0) __builtin_amdgcn_s_setprio(PRIOV); }
  bf16x8 xA[4], wA[3][2], xB[4], wB[3][2];
  auto rd1 = [&](int i, LAS char* sx, LAS char* sw, int hf, bf16x8 (&xf)[4], bf16x8 (&wf)[3][2]) {
    if (i < 4) xf[i] = rd8(sx + offX[hf] + i * 8192);
    else { const int part = (i - 4) >> 1, fb = (i - 4) & 1; wf[part][fb] = cat8(trd(sw + offW[fb] + (32 * hf) * 384 + part * 128), trd(sw + offW[fb] + (32 * hf + 4) * 384 + part * 128)); }
  };
#pragma unroll
  for (int i = 0; i < 10; ++i) rd1(i, lds, lds, 0, xA, wA);
  auto half = [&](auto READ, auto ISSUE, LAS char* sx, LAS char* sw, int hf, unsigned sb, const char* pfp, int pfo, bf16x8 (&xc)[4], bf16x8 (&wc)[3][2], bf16x8 (&xx)[4], bf16x8 (&wx)[3][2]) {
#pragma unroll
    for (int m = 0; m < 24; ++m) {
#define QORD 0
      const int part = m >> 3, b = QORD ? m & 1 : (m >> 2) & 1, tb = QORD ? (m >> 1) & 3 : m & 3;
      if (part == 0) mma16(aq[b][tb], wc[0][b], xc[tb]);
      else if (part == 1) mma16(ak[b][tb], wc[1][b], xc[tb]);
      else mma16(av[tb][b], xc[tb], wc[2][b]);
      SB();
      if constexpr (READ.value) {
#pragma unroll
        for (int i = 0; i < 10; ++i) if (Q_RD[QRV][i] == m) rd1(i, sx, sw, hf, xx, wx);
      }
      if constexpr (ISSUE.value == 1) {
#pragma unroll
        for (int i = 0; i < 3; ++i) if (Q_DW[QDV][i] == m) issueW(sb, i);
        if (m == Q_DW[QDV][3]) {
          pf_if(pfl, pfp, pfo);
        }
      }
      if constexpr (ISSUE.value == 2) {
#pragma unroll
        for (int i = 0; i < 4; ++i) if (Q_DX[QDV][i] == m) issueX(sb, i);
      }
      SB();
    }
  };
  unsigned xa = 0, xb = XS, xc2 = 2 * XS;
  int pfoff = QPF ? __builtin_amdgcn_readfirstlane(wid >> 1) : WPL && (PF16 == 2 || PF16 == 6) ? 1 : __builtin_amdgcn_readfirstlane(wid >> 2);
#pragma unroll
  for (int t = 0; t < NT - 2; ++t) {
    half(IC<1>{}, IC<2>{}, lds + xa, lds + (t & 1) * WS, 1, xc2, nullptr, 0, xA, wA, xB, wB);
    wait_4x(pfoff);
    LBAR();
    pfoff = t < 12 ? __builtin_amdgcn_readfirstlane(((wid >> 1) ^ (t & 3)) | pfn[(t >> 2) % 3]) : 1;
    half(IC<1>{}, IC<1>{}, lds + xb, lds + ((t + 1) & 1) * WS, 0, (t & 1) * WS, pfq[(t >> 2) % 3], pfoff, xB, wB, xA, wA);
    const unsigned x0 = xa; xa = xb; xb = xc2; xc2 = x0;
  }
  { int it = opaque(1); do {
    half(IC<1>{}, IC<0>{}, lds + 2 * XS, lds, 1, 0, nullptr, 0, xA, wA, xB, wB);
    WAIT_V(0); LBAR();
    if (WPL) dma16<0, false>(lane * 16, (const char*)P.wphi + hp * 4096 + (wid & 3) * 1024, LDS_WPHI + (wid & 3) * 1024);
    if (!QPF) { const int te = pfent(QT14); pf_if(pfl, pfadr(te), __builtin_amdgcn_readfirstlane(te == 0xff)); }
    half(IC<1>{}, IC<0>{}, lds, lds + WS, 0, 0, nullptr, 0, xB, wB, xA, wA);
  } while (--it > 0); }
  { int it = opaque(1); do {
    half(IC<1>{}, IC<0>{}, lds, lds + WS, 1, 0, nullptr, 0, xA, wA, xB, wB);
    half(IC<0>{}, IC<0>{}, lds, lds, 0, 0, nullptr, 0, xB, wB, xA, wA);
  } while (--it > 0); }
  MFMA_DONE();
  if (WPL) { if (QPF || pfent(QT14) == 0xff) WAIT_V(0); else WAIT_V(1); }
  LBAR();
  if (PRIO & 2) __builtin_amdgcn_s_setprio(0);
  STAMP(4);
  DELAY(3);
  { const int te = pfent(QT4); if (te != 0xff) pf8k(pfl, pfadr(te)); }
  const unsigned gen = __hip_atomic_load(flagp(P, FL_G), RLX_AGENT);
  const char* hflags = uptr(P.ws + WS_FLAGS + FL_H * 128);
  flags_fetch(lane * 16, hflags, 139264, wid);
  bf16x8 wphi[2];
#pragma unroll
  for (int fb = 0; fb < 2; ++fb) {
    LAS char* a = lds + LDS_WPHI + wn * 2048 + (4 * kg + q) * 64 + fb * 32 + p4 * 8;
    wphi[fb] = cat8(trd(a), trd(a + 16 * 64));
  }
  const f32x4 zero = {0.f, 0.f, 0.f, 0.f};
  auto phi2 = [&](const f32x4 (&acc)[2][4], int tb, u32x4& ap, u32x4& an, u32x4& bp, u32x4& bn) {
    const bf16x8 ka = pack44(acc[0][tb], acc[1][tb]), kb = pack44(acc[0][tb + 1], acc[1][tb + 1]);
    const f32x4 pa[2] = {MFMA16(wphi[0], ka, zero), MFMA16(wphi[1], ka, zero)}, pb[2] = {MFMA16(wphi[0], kb, zero), MFMA16(wphi[1], kb, zero)};
    softmax_pm2(pa, pb, ap, an, bp, bn);
  };
  LAS char* img = lds + wid * 8192;
  auto swz = [](int R) { const int m = (R >> 1) & 7; return ((m & 3) << 2) | ((m >> 2) << 1); };
  {
    const unsigned wb = l15 * 128 + 8 * (kg ^ swz(l15));
#pragma unroll
    for (int tb = 0; tb < 4; tb += 2) {
      u32x4 ph[2][2];
      phi2(ak, tb, ph[0][0], ph[0][1], ph[1][0], ph[1][1]);
#pragma unroll
      for (int e = 0; e < 2; ++e)
#pragma unroll
        for (int fbk = 0; fbk < 4; ++fbk)
          *(LAS u32x2*)(img + (tb + e) * 2048 + (wb ^ (fbk << 5))) = (u32x2){ph[e][fbk >> 1][2 * (fbk & 1)], ph[e][fbk >> 1][2 * (fbk & 1) + 1]};
    }
  }
  WAIT_L0();
  STAMP(16);
  f32x4 kv[4][2], zz[4];
#pragma unroll
  for (int fbk = 0; fbk < 4; ++fbk) { kv[fbk][0] = zero; kv[fbk][1] = zero; zz[fbk] = zero; }
  const bf16x8 ones = mk8(0x3f803f80u, 0x3f803f80u, 0x3f803f80u, 0x3f803f80u);
  {
    const unsigned rb = (4 * kg + q) * 128 + 8 * (p4 ^ swz(4 * kg + q));
#pragma unroll
    for (int ks = 0; ks < 2; ++ks) {
      const bf16x8 vb0 = pack44(av[2 * ks][0], av[2 * ks + 1][0]), vb1 = pack44(av[2 * ks][1], av[2 * ks + 1][1]);
#pragma unroll
      for (int fbk = 0; fbk < 4; ++fbk) {
        LAS char* a = img + ks * 4096 + (rb ^ (fbk << 5));
        const bf16x8 af = cat8(trd(a), trd(a + 2048));
        kv[fbk][0] = MFMA16(af, vb0, kv[fbk][0]); kv[fbk][1] = MFMA16(af, vb1, kv[fbk][1]); zz[fbk] = MFMA16(af, ones, zz[fbk]);
      }
    }
  }
  STAMP(17);
  if (wid == 0) { WAIT_V(0); flags_check(lane * 16, hflags, 139264, (unsigned)__builtin_amdgcn_readfirstlane(gen) + 1u, 0); }
  LAS char* red = lds + 65536;
  constexpr int RS = 8448;
#pragma unroll
  for (int fbk = 0; fbk < 4; ++fbk) {
#pragma unroll
    for (int db = 0; db < 2; ++db) *(LAS f32x4*)(red + wid * RS + (fbk * 2 + db) * 1024 + lane * 16) = kv[fbk][db];
    if (l15 == 0) *(LAS f32x4*)(red + wid * RS + 8192 + fbk * 64 + kg * 16) = zz[fbk];
  }
  LBAR();
  float gt[4];
#pragma unroll
  for (int tb = 0; tb < 4; ++tb) gt[tb] = ld_gate(P, 256 * st + tokb + 16 * tb + l15);
  const int qd = 2 * g + wm2;
  const __amdgpu_buffer_rsrc_t kvrs = rsrc(P.ws + WS_KVP, 2u << 20), zrs = rsrc(P.ws + WS_ZP, 65536);
  {
    f32x4 s2[2];
#pragma unroll
    for (int e = 0; e < 2; ++e) {
      const int ch = 2 * qd + e;
      f32x4 s = (f32x4){0.f, 0.f, 0.f, 0.f};
#pragma unroll
      for (int w = 0; w < 4; ++w) s += *(LAS f32x4*)(red + (2 * w + wn) * RS + ch * 1024 + lane * 16);
      s2[e] = s;
    }
    st16(kvrs, (st * 32 + head) * 4096 + qd * 1024 + lane * 16, (u32x4){pk2(s2[0][0], s2[0][1]), pk2(s2[0][2], s2[0][3]), pk2(s2[1][0], s2[1][1]), pk2(s2[1][2], s2[1][3])});
    if (qd == 0 && lane < 16) {
      f32x4 s = (f32x4){0.f, 0.f, 0.f, 0.f};
#pragma unroll
      for (int w = 0; w < 4; ++w) s += *(LAS f32x4*)(red + (2 * w + wn) * RS + 8192 + lane * 16);
      st16(zrs, ((st * 32 + head) * 64 + lane * 4) * 4, __builtin_bit_cast(u32x4, s));
    }
  }
  WAIT_L0();
  STAMP(18);
  const unsigned nonce = (unsigned)__builtin_amdgcn_readfirstlane(gen) + 1u;
  publish(P, FL_KV + hp * 8 + st, nonce);
  asm volatile("" : "+v"(gt[0]), "+v"(gt[1]), "+v"(gt[2]), "+v"(gt[3]));
  STAMP(5);
  fc_prologue_w(P, fid);
  const FcG fg = fc_gates(P, fid);
  u32x4 phq[4][2];
  phi2(aq, 0, phq[0][0], phq[0][1], phq[1][0], phq[1][1]);
  SB();
  poll_n(P, FL_KV + hp * 8, 8, nonce);
  f32x2 zp = ld8(zrs, (lane >> 3) * (32 * 256) + (head * 64 + 16 * qd + 2 * (lane & 7)) * 4);
  u32x4 a[8];
#pragma unroll
  for (int t = 0; t < 8; ++t) a[t] = __builtin_bit_cast(u32x4, ld16(kvrs, head * 4096 + qd * 1024 + lane * 16 + t * (32 * 4096)));
  SB();
  STAMP(6);
  phi2(aq, 2, phq[2][0], phq[2][1], phq[3][0], phq[3][1]);
  SB();
  STAMP(7);
  LAS char* osc = lds + 131072;
  {
    const int ks = qd, sg = ks >> 1, hf = ks & 1;
    float zx = zp.x, zy = zp.y;
    zx += dpp_mov<0x128>(zx); zy += dpp_mov<0x128>(zy);
    { const f32x2 u = pair16(zx), v = pair16(zy); zx = u.x + u.y; zy = v.x + v.y; }
    { const f32x2 u = pair32(zx), v = pair32(zy); zx = u.x + u.y; zy = v.x + v.y; }
    if (lane < 8) *(LAS unsigned*)(osc + 8192 + (wn * 2 + sg) * 64 + (lane >> 1) * 16 + hf * 8 + (lane & 1) * 4) = pk2(zx, zy);
    float sv[8];
    unsigned clo = 0x00003f80u, chi = 0x3f800000u;
    asm volatile("" : "+s"(clo), "+s"(chi));
#pragma unroll
    for (int j = 0; j < 4; ++j) {
      float lo = bflo(a[0][j]), hi = bfhi(a[0][j]);
#pragma unroll
      for (int t = 1; t < 8; ++t) { lo = dot2(a[t][j], clo, lo); hi = dot2(a[t][j], chi, hi); }
      sv[2 * j] = lo; sv[2 * j + 1] = hi;
    }
#pragma unroll
    for (int db = 0; db < 2; ++db)
      *(LAS u32x2*)(osc + ((wn * 2 + db) * 2 + sg) * 1024 + lane * 16 + hf * 8) = (u32x2){pk2(sv[4 * db], sv[4 * db + 1]), pk2(sv[4 * db + 2], sv[4 * db + 3])};
  }
  STAMP(19);
  const unsigned ulds = LDS_WPHI + 2048 + wid * 256;
  const char* uflags0 = uptr(P.ws + WS_FLAGS + (size_t)fc_first_flag(fid) * 128);
  {
    const int nf = fc_first_count(fid), l = lane & 3;
    asm volatile("s_mov_b32 m0, %2\n\ts_nop 0\n\tglobal_load_lds_dword %0, %1 sc1" ::"v"((l < nf ? l : 0) * 128), "s"(uflags0), "s"(ulds) : "memory");
  }
  int mine = 0;
  LBAR();
  {
    bf16x8 kva[2][2], za[2];
#pragma unroll
    for (int sg = 0; sg < 2; ++sg) {
      za[sg] = rd8(osc + 8192 + (wn * 2 + sg) * 64 + kg * 16);
#pragma unroll
      for (int db = 0; db < 2; ++db) kva[db][sg] = rd8(osc + ((wn * 2 + db) * 2 + sg) * 1024 + lane * 16);
    }
    const __amdgpu_buffer_rsrc_t ors = rsrc(P.ws + WS_OT, 4u << 20);
    const unsigned dst = (head * 128 + 16 * st + (tokb >> 4)) * 1024 + lane * 16;
#pragma unroll
    for (int tb = 0; tb < 4; ++tb) {
      const bf16x8 pp = __builtin_bit_cast(bf16x8, phq[tb][0]), pn = __builtin_bit_cast(bf16x8, phq[tb][1]);
      f32x4 n0 = MFMA16(kva[0][0], pp, zero), n1 = MFMA16(kva[1][0], pp, zero), den = MFMA16(za[0], pp, zero);
      n0 = MFMA16(kva[0][1], pn, n0); n1 = MFMA16(kva[1][1], pn, n1); den = MFMA16(za[1], pn, den);
      if (tb == 2) {
        WAIT_V(2);
        mine = __all(*(LAS unsigned*)(lds + ulds + lane * 4) == nonce);
        if (mine) fc_prologue_u(P, fid);
        if (lane == 0) *(LAS unsigned*)(lds + LDS_CNT + 32 + wid * 4) = mine;
      }
      const float sc = gt[tb] * __builtin_amdgcn_rcpf(den[0] + 1e-6f);
      st16(ors, dst + tb * 1024, (u32x4){pk2(n0[0] * sc, n0[1] * sc), pk2(n0[2] * sc, n0[3] * sc), pk2(n1[0] * sc, n1[1] * sc), pk2(n1[2] * sc, n1[3] * sc)});
    }
  }
  STAMP(20);
  LBAR();
  const int all = __all(*(LAS unsigned*)(lds + LDS_CNT + 32 + (lane & 7) * 4) != 0u);
  STAMP(8);
  return Role{nonce, __builtin_amdgcn_readfirstlane(mine), __builtin_amdgcn_readfirstlane(all), FL_O + st * 16 + hp, fg};
}

DI void fc_role(const Params& P, LAS char* lds, const FcId id, const Role role) {
  using namespace fc;
  const unsigned nonce = role.nonce;
  const int stl = id.stl, dt = id.dt, c = id.c;
  const int tid = threadIdx.x, lane = tid & 63;
  const int wid = __builtin_amdgcn_readfirstlane(tid >> 6);
  const int g = wid >> 2, w4 = wid & 3, wm = w4 >> 1, wn = w4 & 1;
  const int J0 = c ? id.j0 : 0;
  const int rr = 4 * w4 + (lane >> 4), cc = lane & 15;
  const unsigned swz = (((cc >> 1) ^ (rr & 7)) << 5) + ((cc & 1) << 4);
  const unsigned voffW2 = rr * 4096 + swz, voffWo = rr * 2048 + swz, voffL = lane * 16;
  const char* wosrc = (const char*)P.wo + dt * 256;
  const char* osrc = P.ws + WS_OT + (size_t)(stl * 8 + 2 * w4) * 1024;
  const char* wptr = (const char*)P.w2 + (stl < 12 ? WOFF : 0) + stl * 256 + (size_t)(J0 + g) * 32 * 4096;
  const char* lptr = P.ws + WS_UT + (size_t)(J0 + g) * 65536 + (size_t)(dt * 8 + 2 * w4) * 1024;
  if (!role.all) {
    if (wid == 0) poll_p(P, fc_first_flag(id), fc_first_count(id), nonce, LDS_WPHI + 512);
    BAR();
  }
  STAMP(10);
  if (!role.mine) fc_prologue_u(P, id);
  const unsigned mb = g * 65536 + w4 * 4096;
  wptr += 4 * 64 * 4096; lptr += 4 * 2 * 65536;
  const unsigned mbc = mb + 2048;
  const unsigned voffW2a = voffW2 + 2048, voffW2b = voffW2 + 16 * 4096 + 1024, voffWoa = voffWo + 2048, voffWob = voffWo + 16 * 2048 + 1024;
  auto issue1 = [&](auto WO, auto J, int i) {
    constexpr int OFF = J.value * SLOT;
    if constexpr (WO.value != 1) {
      if (i == 0) dma_first<OFF, -2048, false>(voffW2a, wptr, mbc);
      else if (i == 1) { dma_next<-1024, false>(voffW2b, wptr); wptr += 2 * 32 * 4096; }
      else if (i == 2) dma_next<0, true>(voffL, lptr);
      else { dma_next<1024, true>(voffL, lptr); lptr += 2 * 65536; }
    } else {
      if (i == 0) dma_first<OFF, -2048, false>(voffWoa, wptr, mbc);
      else if (i == 1) { dma_next<-1024, false>(voffWob, wptr); wptr += 2 * 32 * 2048; }
      else if (i == 2) dma_next<0, true>(voffL, lptr);
      else { dma_next<1024, true>(voffL, lptr); lptr += 2 * 131072; }
    }
  };
  const int kg = lane >> 4, q = (lane & 15) >> 2, p = lane & 3, r7 = 4 * (kg & 1) + q;
  const unsigned base0 = g * 65536 + kg * 4096 + q * 256 + (r7 << 5) + 8 * p;
  unsigned offTs[4], offTd[4];
#pragma unroll
  for (int i = 0; i < 4; ++i) { offTs[i] = base0 ^ ((4 * wn + (FCEP ? (i + 2 * g) & 3 : i)) << 5); offTd[i] = base0 ^ ((4 * wm + i) << 5); }
  const unsigned offLd = g * 65536 + 2 * wm * 4096 + 2048 + lane * 16, offLs = g * 65536 + 2 * wn * 4096 + 2048 + lane * 16;
  const unsigned offLs0 = offLs + g * 4096, offLs1 = offLs + (1 - g) * 4096;
  float og[4];
#pragma unroll
  for (int i = 0; i < 4; ++i) og[i] = 1.0f - role.fg.og[i];
  const int srow = 128 * stl + 64 * wn + 32 * g + (lane & 15);
  float b2v = bflo(role.fg.b2v), b2w = bflo(role.fg.b2w);
  asm volatile("" : "+v"(og[0]), "+v"(og[1]), "+v"(og[2]), "+v"(og[3]), "+v"(b2v), "+v"(b2w));
  const int pair = stl * 8 + dt;
  GAS unsigned* xflag = flagp(P, FL_X + pair);
  const unsigned xlds = LDS_WPHI + 1024 + wid * 256;

  f32x4 acc[4][4];
  struct Fr { bf16x8 l[4]; s16x4 t[4][2]; };
  Fr fA, fB;
  auto rd1 = [&](auto RK, auto J, int j, Fr& f) {
    LAS char* sl = lds + J.value * SLOT;
    if (j < 4) f.l[j] = rd8(sl + (RK.value == 0 ? offLd + (j >> 1) * 4096 : j < 2 ? offLs0 : offLs1) + (j & 1) * 1024);
    else f.t[(j - 4) >> 1][j & 1] = trd(sl + (RK.value == 0 ? offTs[(j - 4) >> 1] : offTd[(j - 4) >> 1]) + (j & 1) * 1024);
  };
  auto begin = [&]() {
#pragma unroll
    for (int i = 0; i < 4; ++i)
#pragma unroll
      for (int k = 0; k < 4; ++k) acc[i][k] = (f32x4){0.f, 0.f, 0.f, 0.f};
    WAIT_V(0); BAR();
    if (role.late && tid == 0) __hip_atomic_store(flagp(P, role.late), nonce, RLX_AGENT);
    STAMP(11);
    if (PRIO & 4) { if (g == 0) __builtin_amdgcn_s_setprio(PRIOV); }
#pragma unroll
    for (int j = 0; j < 12; ++j) rd1(IC<0>{}, IC<ROT>{}, j, fA);
  };
  const char* uflags = P.ws + WS_FLAGS + (FL_U + dt * 16) * 128;
  auto step = [&](auto J, auto CK, auto RK, auto DK, auto WAITN, Fr& fc_, Fr& fx, int hook = 0, int off = 1, const char* hfl = nullptr) {
    if (hook == 2) { wait_v<WAITN.value < 0 ? 0 : WAITN.value>(); flags16_check(voffL, hfl, LDS_WPHI, nonce, off); }
    wait_vl_bar<WAITN.value>();
    if (hook == 1) flags16_fetch(voffL, hfl, LDS_WPHI, off);
    if (hook == 7 && tid == 0) __hip_atomic_store(flagp(P, role.late), nonce, RLX_AGENT);
#pragma unroll
    for (int m = 0; m < 16; ++m) {
      const int mi = hook == 8 ? (m & 7) >> 1 : m >> 2, mj = hook == 8 ? (m < 8 ? 2 : 0) + (m & 1) : m & 3;
      if (FC_TIED && hook != 8) {
        if constexpr (CK.value == 0) mma16t(acc[mi][mj], fc_.l[mi], cat8(fc_.t[mj][0], fc_.t[mj][1]));
        else mma16t(acc[mi][mj], cat8(fc_.t[mi][0], fc_.t[mi][1]), fc_.l[mj]);
      } else
      if constexpr (CK.value == 0) mma16(acc[mi][mj], fc_.l[mi], cat8(fc_.t[mj][0], fc_.t[mj][1]));
      else mma16(acc[mi][mj], cat8(fc_.t[mi][0], fc_.t[mi][1]), fc_.l[mj]);
      SB();
      if (hook == 8) {
#pragma unroll
        for (int k = 0; k < 8; ++k)
          if ((k + 3 > 2 * k + 1 ? k + 3 : 2 * k + 1) == m) *(LAS f32x4*)(lds + (wid * 8 + k) * 1024 + lane * 16) = acc[k >> 1][2 + (k & 1)];
      }
      if constexpr (RK.value < 2) {
#pragma unroll
        for (int j = 0; j < 12; ++j) if (FC_RD[FRD][j] == m) rd1(RK, IC<(J.value + 1) & 3>{}, j, fx);
      }
      if constexpr (DK.value < 2) {
#pragma unroll
        for (int i = 0; i < 4; ++i) if (FC_DM[FDM][i] == m) issue1(DK, J, i);
      }
      if constexpr (DK.value == 3) {
#pragma unroll
        for (int i = 0; i < 2; ++i) if (FC_DM[FDM][i] == m) issue1(DK, J, i);
      }
      SB();
    }
  };
  auto scale = [&]() {
    MFMA_DONE();
#pragma unroll
    for (int i = 0; i < 4; ++i)
#pragma unroll
      for (int k = 0; k < 4; ++k) acc[i][k] *= og[k];
  };
#define FCW 8
  using I0 = IC<0>; using I1 = IC<1>; using I2 = IC<2>; using I3 = IC<3>; using W8 = IC<FCW>;
  auto four = [&](auto RK, auto RKL, auto DK) {
    step(I0{}, RK, RK, DK, W8{}, fA, fB); step(I1{}, RK, RK, DK, W8{}, fB, fA);
    step(I2{}, RK, RK, DK, W8{}, fA, fB); step(I3{}, RK, RKL, DK, W8{}, fB, fA);
  };
  auto four_first = [&](int off, const char* hfl) {
    step(I0{}, I0{}, I0{}, I0{}, W8{}, fA, fB, 1, off, hfl); step(I1{}, I0{}, I0{}, I0{}, W8{}, fB, fA);
    step(I2{}, I0{}, I0{}, I0{}, W8{}, fA, fB); step(I3{}, I0{}, I0{}, I0{}, W8{}, fB, fA, 2, off, hfl);
  };
  auto tail = [&](auto RK) {
    step(I0{}, RK, RK, I2{}, W8{}, fA, fB); step(I1{}, RK, RK, I2{}, IC<4>{}, fB, fA);
    step(I2{}, RK, RK, I2{}, IC<0>{}, fA, fB);
    SB();
    step(I3{}, RK, I2{}, I2{}, IC<-1>{}, fB, fA, FCEP ? 8 : 0);
  };
  auto two = [&]() { step(I2{}, I0{}, I0{}, I0{}, W8{}, fA, fB); step(I3{}, I0{}, I0{}, I0{}, W8{}, fB, fA); };
  if (c == 0) {
    begin();
    if (ROT) two();
    { int it = (id.j0 >> 3) - 1, off = wid; do { four_first(__builtin_amdgcn_readfirstlane(off), uflags); off = 1; } while (--it > 0); }
    { int it = opaque(1); do { tail(I0{}); } while (--it > 0); }
    MFMA_DONE();
  } else {
    begin();
    if (ROT) two();
    {
      const char* oflags = P.ws + WS_FLAGS + (FL_O + (stl >> 1) * 16) * 128;
      int it = ((112 - id.j0) >> 3) - 1, off = wid; const char* hfl = uflags;
      do { four_first(__builtin_amdgcn_readfirstlane(off), uptr(hfl)); off = it == 2 ? wid : 1; hfl = oflags; } while (--it > 0);
    }
    wptr = wosrc + (size_t)g * (32 * 2048); lptr = osrc + (size_t)g * 131072;
    { int it = opaque(1); do { four(I0{}, I1{}, I1{}); } while (--it > 0); }
    scale();
    { int it = opaque(3); do { four(I1{}, I1{}, I1{}); } while (--it > 0); }
    { int it = opaque(1); do { tail(I1{}); } while (--it > 0); }
  }
  if (PRIO & 4) __builtin_amdgcn_s_setprio(0);
  DELAY(5);
  STAMP(12);
  {
    const __amdgpu_buffer_rsrc_t xb = rsrc(P.ws + WS_XCH + (size_t)pair * 32768, 32768);
    auto fin = [&](auto G) {
      constexpr int gg = FCEP ? 0 : G.value;
      u32x4 px[4];
      if (id.recv) {
#pragma unroll
        for (int k = 0; k < 4; ++k) px[k] = __builtin_bit_cast(u32x4, ld16(xb, (wid * 4 + k) * 1024 + lane * 16));
      }
      LBAR();
      f32x4 keep[2][4];
#pragma unroll
      for (int e = 0; e < 2; ++e)
#pragma unroll
        for (int i = 0; i < 4; ++i) keep[e][i] = acc[i][2 * gg + e] + *(LAS f32x4*)(lds + ((wid ^ 4) * 8 + i * 2 + e) * 1024 + lane * 16);
#pragma unroll
      for (int e = 0; e < 2; ++e) {
        const float fs = c == 0 ? og[e] : 1.0f;
#pragma unroll
        for (int i = 0; i < 4; ++i) keep[e][i] *= fs;
      }
      if (id.recv) {
        for (unsigned spins = 0; spins < SPIN_MAX; ++spins) {
          unsigned mx = 0;
#pragma unroll
          for (int k = 0; k < 4; ++k) mx = umax(umax(mx, umax(px[k][0], px[k][1])), umax(px[k][2], px[k][3]));
          if (!__any(mx == 0xffffffffu)) break;
          __builtin_amdgcn_s_sleep(PXSL);
#pragma unroll
          for (int k = 0; k < 4; ++k) px[k] = __builtin_bit_cast(u32x4, ld16(xb, (wid * 4 + k) * 1024 + lane * 16));
        }
      }
      STAMP(13);
      if (!id.recv) {
#pragma unroll
        for (int e = 0; e < 2; ++e)
#pragma unroll
          for (int i = 0; i < 4; i += 2) {
            const f32x4 a = keep[e][i], b = keep[e][i + 1];
            st16(xb, (wid * 4 + e * 2 + (i >> 1)) * 1024 + lane * 16, (u32x4){umin(pk2(a[0], a[1]), 0xfffffffeu), umin(pk2(a[2], a[3]), 0xfffffffeu), umin(pk2(b[0], b[1]), 0xfffffffeu), umin(pk2(b[2], b[3]), 0xfffffffeu)});
          }
      } else {
        const __amdgpu_buffer_rsrc_t ors = rsrc(P.out, (unsigned)S * D * 2);
        const unsigned orow0 = (unsigned)(srow - (lane & 15));
#pragma unroll
        for (int e = 0; e < 2; ++e) {
          LAS char* ot = lds + 65536 + wid * 4096 + e * 2048;
          const float bias = og[2 * gg + e] * (e ? b2w : b2v);
#pragma unroll
          for (int i = 0; i < 4; i += 2) {
            const u32x4 w = px[e * 2 + (i >> 1)];
            const f32x4 v0 = keep[e][i] + (f32x4){bflo(w[0]), bfhi(w[0]), bflo(w[1]), bfhi(w[1])} + bias;
            const f32x4 v1 = keep[e][i + 1] + (f32x4){bflo(w[2]), bfhi(w[2]), bflo(w[3]), bfhi(w[3])} + bias;
            const auto lo = __builtin_amdgcn_permlane16_swap(pk2(v0[0], v0[1]), pk2(v1[0], v1[1]), false, false);
            const auto hi = __builtin_amdgcn_permlane16_swap(pk2(v0[2], v0[3]), pk2(v1[2], v1[3]), false, false);
            const int c16 = 2 * i + 2 * (kg & 1) + (kg >> 1);
            *(LAS u32x4*)(ot + (lane & 15) * 128 + ((c16 ^ (lane & 7)) << 4)) = (u32x4){lo[0], hi[0], lo[1], hi[1]};
          }
#pragma unroll
          for (int j = 0; j < 2; ++j) {
            const int row = 8 * j + (lane >> 3);
            const u32x4 w = *(LAS u32x4*)(ot + row * 128 + (((lane & 7) ^ (row & 7)) << 4));
            __builtin_amdgcn_raw_buffer_store_b128(w, ors, ((orow0 + 16 * e + row) * D + 128 * dt + 64 * wm) * 2 + (lane & 7) * 16, 0, OUT_AUX);
          }
        }
      }
    };
    fin(I0{});
  }
  STAMP(14);
  STAMP(15);
}

__global__ __launch_bounds__(512, 2) void k_fused(Params P) {
  extern __shared__ __attribute__((aligned(16))) char smem[];
  LAS char* lds = (LAS char*)smem;
  const int b = blockIdx.x, xcd = b & 7, loc = b >> 3, i = loc & 15;
  Role role;
#define CPFL 0
  auto code_pf = [&]() {
    unsigned long long pc; unsigned len;
    asm volatile("s_getpc_b64 %0\n.Lk_first:\n\ts_mov_b32 %1, .Lk_last-.Lk_first" : "=s"(pc), "=s"(len));
    const unsigned off = (unsigned)(loc - CPFL) * 8192 + (threadIdx.x & 63) * 128;
    if ((unsigned)(loc - CPFL) * 8192u < len && (threadIdx.x >> 6) == CODE_PF) pf8k_d(off < len ? off : 0u, (const void*)pc);
  };
  code_pf();
  SB();
#define HMAP 0
#define LNM (HMAP == 0 ? xcd * 16 + i : HMAP == 1 ? (xcd ^ 4) * 16 + i : HMAP == 2 ? ((xcd ^ 4) >> 2) * 64 + i * 4 + (xcd & 3) : i * 8 + (xcd ^ 4))
  LnPre pre;
  ln_issue(P, LNM, loc >> 4, pre);
  qkv_first(P, 4 * ((xcd & 1) ^ QSX) + (i & 3), 4 * (xcd >> 1) + (i >> 2), (loc >> 4) ^ 1);
  SB();
  tlb_warm(P, loc);
#define STLROT 0
  FcId fid; fid.stl = 4 * (xcd & 3) + ((loc + STLROT) & 3); fid.c = xcd >> 2; fid.dt = (loc >> 2) ^ (4 * fid.c); fid.j0 = ROT ? (fid.dt < 4 ? 72 + ASH : 72 - ASH) : fid.dt < 4 ? 72 + 8 * ASY : 72 - 8 * ASY; fid.recv = loc >> 4;
  fid.s0 = 0; fid.own = 1000;
#define MMAP 0
#define MTAB 0xFE92761054DC83BAull
  const int m_dt = MMAP ? i & 7 : 4 * ((xcd & 1) ^ MDX) + (i & 3), m_ft = MMAP ? (int)((unsigned long long)(MTAB) >> (4 * (2 * xcd + (i >> 3)))) & 15 : 4 * (xcd >> 1) + (i >> 2);
  if (loc < 16) role = mlp1_role(P, lds, LNM, m_dt, m_ft, fid, &pre);
  else role = qkv_role16(P, lds, 4 * ((xcd & 1) ^ QSX) + (i & 3), (4 * (xcd >> 1) + (i >> 2)), xcd * 16 + i, fid);
  fc_role(P, lds, fid, role);
  if (b == 255 && threadIdx.x == 0) __hip_atomic_store(flagp(P, FL_G), role.nonce, RLX_AGENT);
  asm volatile(".Lk_last:");
}

extern "C" void kernel_launch(void* const* d_in, const int* in_sizes, int n_in, void* d_out, int out_size, void* d_ws, size_t ws_size, hipStream_t stream) {
  Params P;
  P.x = (const u16*)d_in[0]; P.wqkv = (const u16*)d_in[1]; P.wphi = (const u16*)d_in[2]; P.wo = (const u16*)d_in[3];
  P.w1 = (const u16*)d_in[5]; P.b1 = (const u16*)d_in[6]; P.w2 = (const u16*)d_in[7]; P.b2 = (const u16*)d_in[8]; P.gw = (const u16*)d_in[9];
  P.ws = (char*)d_ws; P.out = (u16*)d_out;
  (void)hipFuncSetAttribute((const void*)k_fused, hipFuncAttributeMaxDynamicSharedMemorySize, LDS_BYTES);
  k_fused<<<256, 512, LDS_BYTES, stream>>>(P);
}
```
